```python
import torch

def _softmax(x, axis=-1):
    return torch.softmax(x, dim=axis)

def _gelu(x):
    return torch.nn.functional.gelu(x, approximate='tanh')

def _silu(x):
    return torch.nn.functional.silu(x)

def _sigmoid(x):
    return torch.sigmoid(x)

def _layernorm(x, axis=-1, eps=1e-05):
    m = x.mean(dim=axis, keepdim=True)
    v = x.var(dim=axis, keepdim=True, unbiased=False)
    return (x - m) / torch.sqrt(v + eps)

def _scatter_add(tgt, idx, src, axis=0):
    return tgt.clone().index_add_(axis, idx, src)

def _segment_sum(src, seg, n):
    out = torch.zeros((n,) + src.shape[1:], dtype=src.dtype)
    return out.index_add_(0, seg, src)

def _pad(x, pw):
    flat = tuple((v for pair in reversed(pw) for v in pair))
    return torch.nn.functional.pad(x, flat)
kernel_name = 'adaLN_cond__compressed_kv_cla__dense_activation_relu__prompt'

def setup_inputs():

    _w = lambda a: (lambda t: t.to(torch.bfloat16) if t.is_floating_point() else t)(torch.as_tensor(a))

    def _A_setup():
        B, H, Sq, Sk, D, M = (1, 32, 2048, 2048, 128, 512)
        Dc = 4 * D
        return {'x': _w((lambda *s: torch.randn(*s, dtype=torch.bfloat16) * 0.05)(B, Sq, M)), 'c_kv': _w((lambda *s: torch.randn(*s, dtype=torch.bfloat16) * 1.6)(B, Sk, Dc)), 'w_q': _w((lambda *s: torch.randn(*s, dtype=torch.bfloat16) * 0.8)(M, H, D) * M ** (-0.5)), 'w_uk': _w((lambda *s: torch.randn(*s, dtype=torch.bfloat16) * 0.8)(Dc, H, D) * Dc ** (-0.5)), 'w_uv': _w((lambda *s: torch.randn(*s, dtype=torch.bfloat16) * 1.6)(Dc, H, D) * Dc ** (-0.5)), 'w_o': _w((lambda *s: torch.randn(*s, dtype=torch.bfloat16) * 1.6)(H, D, M) * (H * D) ** (-0.5))}
    _A_in = _A_setup()

    def _B_setup():
        B, S, D, Dff = (1, 2048, 512, 1792)
        return {'x': _w((lambda *s: torch.randn(*s, dtype=torch.bfloat16) * 0.05)(B, S, D)), 'w1': _w((lambda *s: torch.randn(*s, dtype=torch.bfloat16) * 0.05)(D, Dff)), 'w2': _w((lambda *s: torch.randn(*s, dtype=torch.bfloat16) * 0.05)(Dff, D))}
    _B_in = _B_setup()
    return {'A_x': _A_in['x'], 'A_c_kv': _A_in['c_kv'], 'A_w_q': _A_in['w_q'], 'A_w_uk': _A_in['w_uk'], 'A_w_uv': _A_in['w_uv'], 'A_w_o': _A_in['w_o'], 'B_x': _B_in['x'], 'B_w1': _B_in['w1'], 'B_w2': _B_in['w2'], 'c': _w((lambda *s: torch.randn(*s, dtype=torch.bfloat16) * 0.1)(1, 512) * 8.0), 'w_mod': _w((lambda *s: torch.randn(*s, dtype=torch.bfloat16) * 0.1)(512, 6 * 512) * 512 ** (-0.5) * 8.0)}

def reference(A_x, A_c_kv, A_w_q, A_w_uk, A_w_uv, A_w_o, B_x, B_w1, B_w2, c, w_mod):

    def _A_ref(x, c_kv, w_q, w_uk, w_uv, w_o):
        B, Sq, M = (x.shape[0], x.shape[1], x.shape[2])
        Sk = c_kv.shape[1]
        H, D = (w_q.shape[1], w_q.shape[2])
        q = torch.einsum('bqm,mhd->bhqd', x, w_q)
        k = torch.einsum('bkc,chd->bhkd', c_kv, w_uk)
        v = torch.einsum('bkc,chd->bhkd', c_kv, w_uv)
        scores = torch.einsum('bhqd,bhkd->bhqk', q, k) * D ** (-0.5)
        off = Sk - Sq
        qi = torch.arange(Sq)[:, None]
        kj = torch.arange(Sk)[None, :]
        cmask = kj <= qi + off
        scores = torch.where(cmask[None, None], scores, -1000000000.0)
        p = _softmax(scores, axis=-1)
        ctx = torch.einsum('bhqk,bhkd->bhqd', p, v)
        return torch.einsum('bhqd,hdm->bqm', ctx, w_o)

    def _B_ref(x, w1, w2):
        h = torch.einsum('bsd,df->bsf', x, w1)
        zero = torch.zeros_like(h)
        h = torch.maximum(h, zero)
        return torch.einsum('bsf,fd->bsd', h, w2)
    x = A_x
    mod = (_silu(c) @ w_mod).reshape(c.shape[0], 6, 1, -1)
    sa, ca, ga, sb, cb, gb = (mod[:, 0].clone(), mod[:, 1].clone(), mod[:, 2].clone(), mod[:, 3].clone(), mod[:, 4].clone(), mod[:, 5].clone())
    h = _layernorm(x, axis=-1) * (1 + ca) + sa
    x = x + ga * _A_ref(h, A_c_kv, A_w_q, A_w_uk, A_w_uv, A_w_o)
    h = _layernorm(x, axis=-1) * (1 + cb) + sb
    x = x + gb * _B_ref(h, B_w1, B_w2)
    return x
```

```cpp
#include <hip/hip_runtime.h>
#include <hip/hip_bf16.h>
#include <stdint.h>

typedef unsigned short bf16_t;
typedef short s16x4 __attribute__((ext_vector_type(4)));
typedef short bf16x8 __attribute__((ext_vector_type(8)));
typedef float f32x16 __attribute__((ext_vector_type(16)));
typedef float f32x4 __attribute__((ext_vector_type(4)));
typedef float f32x2 __attribute__((ext_vector_type(2)));
typedef unsigned u32x4 __attribute__((ext_vector_type(4)));
typedef unsigned u32x2 __attribute__((ext_vector_type(2)));
typedef __bf16 bfx2 __attribute__((ext_vector_type(2)));
typedef __attribute__((address_space(3))) s16x4* lds_s16x4_ptr;
typedef __attribute__((address_space(3))) unsigned* lds_u32_ptr;

#define DI __device__ __forceinline__

constexpr int S_ = 2048, M_ = 512, H_ = 32, D_ = 128, HD_ = 4096, FF_ = 1792;

DI unsigned pk_bf16(float lo, float hi) {
  f32x2 v = {lo, hi};
  bfx2 r = __builtin_convertvector(v, bfx2);
  return __builtin_bit_cast(unsigned, r);
}
DI float bf_lo(unsigned u) { return __uint_as_float(u << 16); }
DI float bf_hi(unsigned u) { return __uint_as_float(u & 0xffff0000u); }
DI float bf2f(bf16_t v) { return __uint_as_float(((unsigned)v) << 16); }
DI float rbf(float x) { return bf_lo(pk_bf16(x, 0.f)); }
DI f32x16 mfma32(bf16x8 a, bf16x8 b, f32x16 c) { return __builtin_amdgcn_mfma_f32_32x32x16_bf16(a, b, c, 0, 0, 0); }
DI bf16x8 tr2(const char* p0, const char* p1) {
  s16x4 a = __builtin_amdgcn_ds_read_tr16_b64_v4i16((lds_s16x4_ptr)(p0));
  s16x4 b = __builtin_amdgcn_ds_read_tr16_b64_v4i16((lds_s16x4_ptr)(p1));
  return __builtin_shufflevector(a, b, 0, 1, 2, 3, 4, 5, 6, 7);
}
DI void glds16(const void* g, char* l) {
  __builtin_amdgcn_global_load_lds((const unsigned*)g, (lds_u32_ptr)l, 16, 0, 0);
}

__global__ __launch_bounds__(256) void mod_kernel(const bf16_t* __restrict__ c, const bf16_t* __restrict__ wmod, float* __restrict__ mod) {
  __shared__ float red[32][64];
  const int tid = threadIdx.x, cg = tid & 7, rg = tid >> 3, n0 = blockIdx.x * 64 + cg * 8;
  float acc[8] = {0, 0, 0, 0, 0, 0, 0, 0};
#pragma unroll
  for (int i = 0; i < 16; ++i) {
    const int k = rg + 32 * i;
    float cv = bf2f(c[k]);
    float s = rbf(cv / (1.f + __expf(-cv)));
    u32x4 w = *(const u32x4*)(wmod + (size_t)k * 3072 + n0);
#pragma unroll
    for (int j = 0; j < 4; ++j) { acc[2 * j] += s * bf_lo(w[j]); acc[2 * j + 1] += s * bf_hi(w[j]); }
  }
#pragma unroll
  for (int j = 0; j < 8; ++j) red[rg][cg * 8 + j] = acc[j];
  __syncthreads();
  if (tid < 64) {
    float s = 0;
#pragma unroll
    for (int i = 0; i < 32; ++i) s += red[i][tid];
    mod[blockIdx.x * 64 + tid] = rbf(s);
  }
}

__global__ __launch_bounds__(256) void ln_kernel(const bf16_t* __restrict__ x, const float* __restrict__ shift, const float* __restrict__ scale, bf16_t* __restrict__ h) {
  const int lane = threadIdx.x & 63, row = blockIdx.x * 4 + (threadIdx.x >> 6);
  u32x4 v = *(const u32x4*)(x + (size_t)row * M_ + lane * 8);
  float f[8];
#pragma unroll
  for (int j = 0; j < 4; ++j) { f[2 * j] = bf_lo(v[j]); f[2 * j + 1] = bf_hi(v[j]); }
  float s = 0;
#pragma unroll
  for (int j = 0; j < 8; ++j) s += f[j];
#pragma unroll
  for (int o = 32; o; o >>= 1) s += __shfl_xor(s, o);
  const float mean = s * (1.f / M_);
  float q = 0;
#pragma unroll
  for (int j = 0; j < 8; ++j) { f[j] -= mean; q += f[j] * f[j]; }
#pragma unroll
  for (int o = 32; o; o >>= 1) q += __shfl_xor(q, o);
  const float rstd = rsqrtf(q * (1.f / M_) + 1e-5f);
  u32x4 o;
#pragma unroll
  for (int j = 0; j < 4; ++j) {
    const int n = lane * 8 + 2 * j;
    float a = f[2 * j] * rstd * rbf(1.f + scale[n]) + shift[n];
    float b = f[2 * j + 1] * rstd * rbf(1.f + scale[n + 1]) + shift[n + 1];
    o[j] = pk_bf16(a, b);
  }
  *(u32x4*)(h + (size_t)row * M_ + lane * 8) = o;
}

enum { EPI_PLAIN = 0, EPI_RELU = 1, EPI_RES = 2 };
struct GemmArgs {
  const bf16_t* A; const bf16_t* B; bf16_t* C;
  const bf16_t* res; const float* gate;
  int lda, ldb, ldc, K;
};

template <int BM, int BN, int WM, int WN, int EPI>
DI void gemm_body(const GemmArgs& g, int m0, int n0, char* smem) {
  constexpr int NT = WM * WN * 64, NW = WM * WN;
  constexpr int WTM = BM / WM, WTN = BN / WN, TM = WTM / 32, TN = WTN / 32;
  constexpr int A_BYTES = BM * 128, B_BYTES = 64 * BN * 2, BUF = A_BYTES + B_BYTES;
  constexpr int A_INSTR = A_BYTES / 1024, B_INSTR = B_BYTES / 1024;
  static_assert(A_INSTR % NW == 0 && B_INSTR % NW == 0, "staging split");
  const int tid = threadIdx.x, lane = tid & 63, w = __builtin_amdgcn_readfirstlane(tid >> 6);
  const int wm = w / WN, wn = w % WN, r32 = lane & 31, hi = lane >> 5;

  const bf16_t* asrc[A_INSTR / NW];
  const bf16_t* bsrc[B_INSTR / NW];
#pragma unroll
  for (int i = 0; i < A_INSTR / NW; ++i) {
    const int piece = w + i * NW, r = piece * 8 + (lane >> 3), ch = (lane & 7) ^ ((r >> 1) & 7);
    asrc[i] = g.A + (size_t)(m0 + r) * g.lda + ch * 8;
  }
  constexpr int UPR = BN / 8;
#pragma unroll
  for (int i = 0; i < B_INSTR / NW; ++i) {
    const int piece = w + i * NW, u = piece * 64 + lane, r = u / UPR, uu = u % UPR;
    const int c = (uu >> 2) ^ (BN >= 128 ? (r & 3) : ((r >> 1) & 1));
    bsrc[i] = g.B + (size_t)r * g.ldb + n0 + (c * 4 + (uu & 3)) * 8;
  }
  auto stage = [&](int t, int buf) {
    char* base = smem + buf * BUF;
#pragma unroll
    for (int i = 0; i < A_INSTR / NW; ++i) glds16(asrc[i] + t * 64, base + (w + i * NW) * 1024);
#pragma unroll
    for (int i = 0; i < B_INSTR / NW; ++i) glds16(bsrc[i] + (size_t)t * 64 * g.ldb, base + A_BYTES + (w + i * NW) * 1024);
  };

  f32x16 acc[TM][TN];
#pragma unroll
  for (int i = 0; i < TM; ++i)
#pragma unroll
    for (int j = 0; j < TN; ++j)
#pragma unroll
      for (int r = 0; r < 16; ++r) acc[i][j][r] = 0.f;

  const int asw = (lane >> 1) & 7;
  const int a_off = (wm * WTM + r32) * 128;
  const int q = (lane & 15) >> 2, p = lane & 3, chalf = (lane >> 4) & 1;
  const int bsw = BN >= 128 ? q : (q >> 1);
  const int b_row = (8 * hi + q) * (BN * 2) + (chalf * 16 + 4 * p) * 2;
  const int nt = g.K / 64;
  stage(0, 0);
  for (int t = 0; t < nt; ++t) {
    __syncthreads();
    if (t + 1 < nt) stage(t + 1, (t + 1) & 1);
    const char* As = smem + (t & 1) * BUF;
    const char* Bs = As + A_BYTES;
#pragma unroll
    for (int s = 0; s < 4; ++s) {
      bf16x8 a[TM], b[TN];
#pragma unroll
      for (int i = 0; i < TM; ++i) a[i] = *(const bf16x8*)(As + a_off + i * 32 * 128 + (((2 * s + hi) ^ asw) << 4));
#pragma unroll
      for (int j = 0; j < TN; ++j) {
        const int cj = (wn * WTN) / 32 + j;
        const char* pb = Bs + b_row + ((cj ^ bsw) << 6) + (16 * s) * (BN * 2);
        b[j] = tr2(pb, pb + 4 * (BN * 2));
      }
#pragma unroll
      for (int i = 0; i < TM; ++i)
#pragma unroll
        for (int j = 0; j < TN; ++j) acc[i][j] = mfma32(b[j], a[i], acc[i][j]);
    }
  }
#pragma unroll
  for (int i = 0; i < TM; ++i) {
    const int m = m0 + wm * WTM + i * 32 + r32;
#pragma unroll
    for (int j = 0; j < TN; ++j) {
#pragma unroll
      for (int gq = 0; gq < 4; ++gq) {
        const int n = n0 + wn * WTN + j * 32 + 8 * gq + 4 * hi;
        float v0 = acc[i][j][4 * gq], v1 = acc[i][j][4 * gq + 1], v2 = acc[i][j][4 * gq + 2], v3 = acc[i][j][4 * gq + 3];
        if (EPI == EPI_RELU) { v0 = fmaxf(v0, 0.f); v1 = fmaxf(v1, 0.f); v2 = fmaxf(v2, 0.f); v3 = fmaxf(v3, 0.f); }
        if (EPI == EPI_RES) {
          u32x2 rr = *(const u32x2*)(g.res + (size_t)m * g.ldc + n);
          f32x4 gt = *(const f32x4*)(g.gate + n);
          v0 = bf_lo(rr[0]) + rbf(gt[0] * rbf(v0));
          v1 = bf_hi(rr[0]) + rbf(gt[1] * rbf(v1));
          v2 = bf_lo(rr[1]) + rbf(gt[2] * rbf(v2));
          v3 = bf_hi(rr[1]) + rbf(gt[3] * rbf(v3));
        }
        u32x2 o = {pk_bf16(v0, v1), pk_bf16(v2, v3)};
        *(u32x2*)(g.C + (size_t)m * g.ldc + n) = o;
      }
    }
  }
}

template <int BM, int BN, int WM, int WN, int EPI>
__global__ __launch_bounds__(WM* WN * 64) void gemm_kernel(GemmArgs g, int ntn) {
  extern __shared__ __attribute__((aligned(16))) char smem[];
  const int bm = blockIdx.x / ntn, bn = blockIdx.x % ntn;
  gemm_body<BM, BN, WM, WN, EPI>(g, bm * BM, bn * BN, smem);
}

template <int BM, int BN, int WM, int WN>
__global__ __launch_bounds__(WM* WN * 64) void qkv_kernel(const bf16_t* h, const bf16_t* ckv, const bf16_t* wq, const bf16_t* wk, const bf16_t* wv,
                                                           bf16_t* q, bf16_t* k, bf16_t* v) {
  extern __shared__ __attribute__((aligned(16))) char smem[];
  constexpr int NTN = HD_ / BN, NTM = S_ / BM, PER = NTN * NTM;
  const int which = blockIdx.x / PER, b = blockIdx.x % PER;
  GemmArgs g;
  g.A = which == 0 ? h : ckv;
  g.B = which == 0 ? wq : (which == 1 ? wk : wv);
  g.C = which == 0 ? q : (which == 1 ? k : v);
  g.res = nullptr; g.gate = nullptr; g.lda = M_; g.ldb = HD_; g.ldc = HD_; g.K = M_;
  gemm_body<BM, BN, WM, WN, EPI_PLAIN>(g, (b / NTN) * BM, (b % NTN) * BN, smem);
}

__global__ __launch_bounds__(512) void attn_kernel(const bf16_t* __restrict__ Q, const bf16_t* __restrict__ Kg, const bf16_t* __restrict__ Vg, bf16_t* __restrict__ ctx) {
  extern __shared__ __attribute__((aligned(16))) char smem[];
  const int tid = threadIdx.x, lane = tid & 63, w = __builtin_amdgcn_readfirstlane(tid >> 6), g = w >> 2, wq = w & 3;
  const int r32 = lane & 31, hi = lane >> 5;
  const int b = blockIdx.x, head = (b & 7) + 8 * (b >> 6), pair = (b >> 3) & 7;
  char* Kl = smem + g * 65536;
  char* Vl = Kl + 32768;
  const int gt = tid & 255, srow = gt >> 4, sch = gt & 15;
  const int k_wr = srow * 256 + ((sch ^ (srow & 15)) << 4);
  const int v_wr = srow * 256 + (((((sch >> 2) ^ (srow & 3)) << 2) | (sch & 3)) << 4);
  const size_t g_off = (size_t)srow * HD_ + head * D_ + sch * 8;
  const int ksw = r32 & 15;
  const int q4 = (lane & 15) >> 2, p4 = lane & 3, chalf = (lane >> 4) & 1;
  const int v_rd = (4 * hi + q4) * 256 + (chalf * 16 + 4 * p4) * 2;
  constexpr float C2 = 0.08838834764831845f * 1.4426950408889634f;

  for (int pass = 0; pass < 2; ++pass) {
    const int x = pass == 0 ? 15 - pair : pair;
    const int nt = x + 1;
    const int qrow = 128 * x + 32 * wq + r32;
    bf16x8 qr[8];
#pragma unroll
    for (int s = 0; s < 8; ++s) qr[s] = *(const bf16x8*)(Q + (size_t)qrow * HD_ + head * D_ + 16 * s + 8 * hi);
    u32x4 sk[4], sv[4];
    {
      const size_t base = (size_t)(64 * g) * HD_ + g_off;
#pragma unroll
      for (int i = 0; i < 4; ++i) { sk[i] = *(const u32x4*)(Kg + base + (size_t)16 * i * HD_); sv[i] = *(const u32x4*)(Vg + base + (size_t)16 * i * HD_); }
#pragma unroll
      for (int i = 0; i < 4; ++i) { *(u32x4*)(Kl + k_wr + i * 4096) = sk[i]; *(u32x4*)(Vl + v_wr + i * 4096) = sv[i]; }
    }
    __syncthreads();
    float m = -1e30f, l = 0.f;
    f32x16 o[4];
#pragma unroll
    for (int d = 0; d < 4; ++d)
#pragma unroll
      for (int r = 0; r < 16; ++r) o[d][r] = 0.f;

    for (int it = 0; it < nt; ++it) {
      const int buf = it & 1;
      const bool last = it + 1 == nt;
      if (!last) {
        const size_t base = (size_t)(64 * (2 * (it + 1) + g)) * HD_ + g_off;
#pragma unroll
        for (int i = 0; i < 4; ++i) { sk[i] = *(const u32x4*)(Kg + base + (size_t)16 * i * HD_); sv[i] = *(const u32x4*)(Vg + base + (size_t)16 * i * HD_); }
      }
      if (!(last && g == 1 && wq < 2)) {
        const char* Kb = Kl + buf * 16384;
        const char* Vb = Vl + buf * 16384;
        f32x16 s0, s1;
#pragma unroll
        for (int r = 0; r < 16; ++r) { s0[r] = 0.f; s1[r] = 0.f; }
#pragma unroll
        for (int s = 0; s < 8; ++s) {
          const int off = r32 * 256 + (((2 * s + hi) ^ ksw) << 4);
          bf16x8 k0 = *(const bf16x8*)(Kb + off);
          bf16x8 k1 = *(const bf16x8*)(Kb + off + 32 * 256);
          s0 = mfma32(k0, qr[s], s0);
          s1 = mfma32(k1, qr[s], s1);
        }
        if (last) {
          const int dq = qrow - (64 * (2 * it + g)) - 4 * hi;
#pragma unroll
          for (int r = 0; r < 16; ++r) {
            const int c = (r & 3) + 8 * (r >> 2);
            if (c > dq) s0[r] = -__builtin_inff();
            if (c + 32 > dq) s1[r] = -__builtin_inff();
          }
        }
        float mx = s0[0];
#pragma unroll
        for (int r = 1; r < 16; ++r) mx = fmaxf(mx, s0[r]);
#pragma unroll
        for (int r = 0; r < 16; ++r) mx = fmaxf(mx, s1[r]);
        mx = fmaxf(mx, __shfl_xor(mx, 32));
        const float mnew = fmaxf(m, mx);
        const float alpha = __builtin_amdgcn_exp2f((m - mnew) * C2);
        m = mnew;
        const float mc = -mnew * C2;
        float ps = 0.f;
#pragma unroll
        for (int r = 0; r < 16; ++r) { s0[r] = __builtin_amdgcn_exp2f(fmaf(s0[r], C2, mc)); ps += s0[r]; }
#pragma unroll
        for (int r = 0; r < 16; ++r) { s1[r] = __builtin_amdgcn_exp2f(fmaf(s1[r], C2, mc)); ps += s1[r]; }
        l = l * alpha + ps;
        if (__any(alpha != 1.f)) {
#pragma unroll
          for (int d = 0; d < 4; ++d)
#pragma unroll
            for (int r = 0; r < 16; ++r) o[d][r] *= alpha;
        }
#pragma unroll
        for (int s = 0; s < 4; ++s) {
          u32x4 pw;
#pragma unroll
          for (int j = 0; j < 4; ++j) {
            const int r = 8 * (s & 1) + 2 * j;
            pw[j] = s < 2 ? pk_bf16(s0[r], s0[r + 1]) : pk_bf16(s1[r], s1[r + 1]);
          }
          const bf16x8 pb = __builtin_bit_cast(bf16x8, pw);
#pragma unroll
          for (int d = 0; d < 4; ++d) {
            const char* pv = Vb + v_rd + 16 * s * 256 + ((d ^ q4) << 6);
            bf16x8 va = tr2(pv, pv + 8 * 256);
            o[d] = mfma32(va, pb, o[d]);
          }
        }
      }
      if (!last) {
        char* Kn = Kl + (buf ^ 1) * 16384;
        char* Vn = Vl + (buf ^ 1) * 16384;
#pragma unroll
        for (int i = 0; i < 4; ++i) { *(u32x4*)(Kn + k_wr + i * 4096) = sk[i]; *(u32x4*)(Vn + v_wr + i * 4096) = sv[i]; }
      }
      __syncthreads();
    }
    l += __shfl_xor(l, 32);
    float* xo = (float*)(smem + w * 8192);
    float* st = (float*)(smem + 65536 + w * 512);
#pragma unroll
    for (int dd = 0; dd < 2; ++dd)
#pragma unroll
      for (int r = 0; r < 16; ++r) xo[(dd * 16 + r) * 64 + lane] = g == 0 ? o[2 + dd][r] : o[dd][r];
    st[lane] = m; st[64 + lane] = l;
    __syncthreads();
    {
      const float* po = (const float*)(smem + (w ^ 4) * 8192);
      const float* pst = (const float*)(smem + 65536 + (w ^ 4) * 512);
      const float mo = pst[lane], lo = pst[64 + lane];
      const float mt = fmaxf(m, mo);
      const float a1 = __builtin_amdgcn_exp2f((m - mt) * C2), a2 = __builtin_amdgcn_exp2f((mo - mt) * C2);
      const float inv = 1.f / (l * a1 + lo * a2);
      const float f1 = a1 * inv, f2 = a2 * inv;
      bf16_t* orow = ctx + (size_t)qrow * HD_ + head * D_ + g * 64;
#pragma unroll
      for (int dd = 0; dd < 2; ++dd)
#pragma unroll
        for (int gq = 0; gq < 4; ++gq) {
          float v[4];
#pragma unroll
          for (int e = 0; e < 4; ++e) {
            const int r = 4 * gq + e;
            const float own = g == 0 ? o[dd][r] : o[2 + dd][r];
            v[e] = own * f1 + po[(dd * 16 + r) * 64 + lane] * f2;
          }
          u32x2 ov = {pk_bf16(v[0], v[1]), pk_bf16(v[2], v[3])};
          *(u32x2*)(orow + dd * 32 + 8 * gq + 4 * hi) = ov;
        }
    }
    __syncthreads();
  }
}

extern "C" void kernel_launch(void* const* d_in, const int* in_sizes, int n_in,
                              void* d_out, int out_size, void* d_ws, size_t ws_size,
                              hipStream_t stream) {
  const bf16_t* x = (const bf16_t*)d_in[0];
  const bf16_t* ckv = (const bf16_t*)d_in[1];
  const bf16_t* wq = (const bf16_t*)d_in[2];
  const bf16_t* wuk = (const bf16_t*)d_in[3];
  const bf16_t* wuv = (const bf16_t*)d_in[4];
  const bf16_t* wo = (const bf16_t*)d_in[5];
  const bf16_t* w1 = (const bf16_t*)d_in[7];
  const bf16_t* w2 = (const bf16_t*)d_in[8];
  const bf16_t* c = (const bf16_t*)d_in[9];
  const bf16_t* wmod = (const bf16_t*)d_in[10];
  bf16_t* out = (bf16_t*)d_out;

  char* ws = (char*)d_ws;
  float* mod = (float*)ws;
  bf16_t* h1 = (bf16_t*)(ws + (1 << 20));
  bf16_t* x1 = (bf16_t*)(ws + (4 << 20));
  bf16_t* h2 = (bf16_t*)(ws + (7 << 20));
  bf16_t* hid = (bf16_t*)(ws + (10 << 20));
  bf16_t* q = (bf16_t*)(ws + (32 << 20));
  bf16_t* k = (bf16_t*)(ws + (48 << 20));
  bf16_t* v = (bf16_t*)(ws + (64 << 20));
  bf16_t* ctx = (bf16_t*)(ws + (80 << 20));

  mod_kernel<<<48, 256, 0, stream>>>(c, wmod, mod);
  ln_kernel<<<S_ / 4, 256, 0, stream>>>(x, mod + 0 * 512, mod + 1 * 512, h1);
  {
    constexpr int BM = 128, BN = 128, WM = 2, WN = 2, SH = 2 * (BM * 128 + 64 * BN * 2);
    hipFuncSetAttribute((const void*)qkv_kernel<BM, BN, WM, WN>, hipFuncAttributeMaxDynamicSharedMemorySize, SH);
    qkv_kernel<BM, BN, WM, WN><<<3 * (S_ / BM) * (HD_ / BN), WM * WN * 64, SH, stream>>>(h1, ckv, wq, wuk, wuv, q, k, v);
  }
  hipFuncSetAttribute((const void*)attn_kernel, hipFuncAttributeMaxDynamicSharedMemorySize, 131072);
  attn_kernel<<<256, 512, 131072, stream>>>(q, k, v, ctx);
  {
    constexpr int BM = 64, BN = 64, WM = 2, WN = 2, SH = 2 * (BM * 128 + 64 * BN * 2);
    GemmArgs g; g.A = ctx; g.B = wo; g.C = x1; g.res = x; g.gate = mod + 2 * 512; g.lda = HD_; g.ldb = M_; g.ldc = M_; g.K = HD_;
    gemm_kernel<BM, BN, WM, WN, EPI_RES><<<(S_ / BM) * (M_ / BN), WM * WN * 64, SH, stream>>>(g, M_ / BN);
  }
  ln_kernel<<<S_ / 4, 256, 0, stream>>>(x1, mod + 3 * 512, mod + 4 * 512, h2);
  {
    constexpr int BM = 128, BN = 128, WM = 2, WN = 2, SH = 2 * (BM * 128 + 64 * BN * 2);
    GemmArgs g; g.A = h2; g.B = w1; g.C = hid; g.res = nullptr; g.gate = nullptr; g.lda = M_; g.ldb = FF_; g.ldc = FF_; g.K = M_;
    hipFuncSetAttribute((const void*)gemm_kernel<BM, BN, WM, WN, EPI_RELU>, hipFuncAttributeMaxDynamicSharedMemorySize, SH);
    gemm_kernel<BM, BN, WM, WN, EPI_RELU><<<(S_ / BM) * (FF_ / BN), WM * WN * 64, SH, stream>>>(g, FF_ / BN);
  }
  {
    constexpr int BM = 64, BN = 64, WM = 2, WN = 2, SH = 2 * (BM * 128 + 64 * BN * 2);
    GemmArgs g; g.A = hid; g.B = w2; g.C = out; g.res = x1; g.gate = mod + 5 * 512; g.lda = FF_; g.ldb = M_; g.ldc = M_; g.K = FF_;
    gemm_kernel<BM, BN, WM, WN, EPI_RES><<<(S_ / BM) * (M_ / BN), WM * WN * 64, SH, stream>>>(g, M_ / BN);
  }
}
```

```cpp
#include <hip/hip_runtime.h>
#include <hip/hip_bf16.h>
#include <stdint.h>

typedef unsigned short bf16_t;
typedef short s16x4 __attribute__((ext_vector_type(4)));
typedef short bf16x8 __attribute__((ext_vector_type(8)));
typedef float f32x16 __attribute__((ext_vector_type(16)));
typedef float f32x4 __attribute__((ext_vector_type(4)));
typedef float f32x2 __attribute__((ext_vector_type(2)));
typedef unsigned u32x4 __attribute__((ext_vector_type(4)));
typedef unsigned u32x2 __attribute__((ext_vector_type(2)));
typedef __bf16 bfx2 __attribute__((ext_vector_type(2)));
typedef __attribute__((address_space(3))) s16x4* lds_s16x4_ptr;
typedef __attribute__((address_space(3))) unsigned* lds_u32_ptr;

#if defined(__HIP_DEVICE_COMPILE__)
#define NOPK __attribute__((target("no-packed-fp32-ops")))
#else
#define NOPK
#endif
#define DI NOPK __device__ __forceinline__

constexpr int S_ = 2048, M_ = 512, H_ = 32, D_ = 128, HD_ = 4096, FF_ = 1792;

DI float u2f(unsigned u) { return __builtin_bit_cast(float, u); }
DI unsigned f2u(float f) { return __builtin_bit_cast(unsigned, f); }
#define SYNC() asm volatile("s_waitcnt vmcnt(0) lgkmcnt(0)\n\ts_barrier" ::: "memory")
DI float shx(float v, int o) {
  const int lane = __builtin_amdgcn_mbcnt_hi(~0u, __builtin_amdgcn_mbcnt_lo(~0u, 0u));
  return u2f((unsigned)__builtin_amdgcn_ds_bpermute((lane ^ o) << 2, (int)f2u(v)));
}
DI bool any_lane(bool p) { return __builtin_amdgcn_ballot_w64(p) != 0ull; }
DI unsigned pk_bf16(float lo, float hi) {
  f32x2 v = {lo, hi};
  bfx2 r = __builtin_convertvector(v, bfx2);
  return __builtin_bit_cast(unsigned, r);
}
DI float bf_lo(unsigned u) { return u2f(u << 16); }
DI float bf_hi(unsigned u) { return u2f(u & 0xffff0000u); }
DI float bf2f(bf16_t v) { return u2f(((unsigned)v) << 16); }
DI float rbf(float x) { return bf_lo(pk_bf16(x, 0.f)); }
DI f32x16 mfma32(bf16x8 a, bf16x8 b, f32x16 c) { return __builtin_amdgcn_mfma_f32_32x32x16_bf16(a, b, c, 0, 0, 0); }
#define PIN(x) asm volatile("" : "+v"(x))
DI bf16x8 tr2(const char* p0, const char* p1) {
  s16x4 a = __builtin_amdgcn_ds_read_tr16_b64_v4i16((lds_s16x4_ptr)(p0));
  s16x4 b = __builtin_amdgcn_ds_read_tr16_b64_v4i16((lds_s16x4_ptr)(p1));
  return __builtin_shufflevector(a, b, 0, 1, 2, 3, 4, 5, 6, 7);
}
DI void glds16(const void* g, char* l) {
  __builtin_amdgcn_global_load_lds((const unsigned*)g, (lds_u32_ptr)l, 16, 0, 0);
}

NOPK __global__ __launch_bounds__(256) void mod_kernel(const bf16_t* __restrict__ c, const bf16_t* __restrict__ wmod, float* __restrict__ mod) {
  __shared__ float red[32][64];
  const int tid = threadIdx.x, cg = tid & 7, rg = tid >> 3, n0 = blockIdx.x * 64 + cg * 8;
  float acc[8] = {0, 0, 0, 0, 0, 0, 0, 0};
#pragma unroll
  for (int i = 0; i < 16; ++i) {
    const int k = rg + 32 * i;
    float cv = bf2f(c[k]);
    float s = rbf(cv / (1.f + __expf(-cv)));
    u32x4 w = *(const u32x4*)(wmod + (size_t)k * 3072 + n0);
#pragma unroll
    for (int j = 0; j < 4; ++j) { acc[2 * j] += s * bf_lo(w[j]); acc[2 * j + 1] += s * bf_hi(w[j]); }
  }
#pragma unroll
  for (int j = 0; j < 8; ++j) red[rg][cg * 8 + j] = acc[j];
  SYNC();
  if (tid < 64) {
    float s = 0;
#pragma unroll
    for (int i = 0; i < 32; ++i) s += red[i][tid];
    mod[blockIdx.x * 64 + tid] = rbf(s);
  }
}

NOPK __global__ __launch_bounds__(256) void ln_kernel(const bf16_t* __restrict__ x, const float* __restrict__ shift, const float* __restrict__ scale, bf16_t* __restrict__ h) {
  const int lane = threadIdx.x & 63, row = blockIdx.x * 4 + (threadIdx.x >> 6);
  u32x4 v = *(const u32x4*)(x + (size_t)row * M_ + lane * 8);
  float f[8];
#pragma unroll
  for (int j = 0; j < 4; ++j) { f[2 * j] = bf_lo(v[j]); f[2 * j + 1] = bf_hi(v[j]); }
  float s = 0;
#pragma unroll
  for (int j = 0; j < 8; ++j) s += f[j];
#pragma unroll
  for (int o = 32; o; o >>= 1) s += shx(s, o);
  const float mean = s * (1.f / M_);
  float q = 0;
#pragma unroll
  for (int j = 0; j < 8; ++j) { f[j] -= mean; q += f[j] * f[j]; }
#pragma unroll
  for (int o = 32; o; o >>= 1) q += shx(q, o);
  const float rstd = rsqrtf(q * (1.f / M_) + 1e-5f);
  u32x4 o;
#pragma unroll
  for (int j = 0; j < 4; ++j) {
    const int n = lane * 8 + 2 * j;
    float a = f[2 * j] * rstd * rbf(1.f + scale[n]) + shift[n];
    float b = f[2 * j + 1] * rstd * rbf(1.f + scale[n + 1]) + shift[n + 1];
    o[j] = pk_bf16(a, b);
  }
  *(u32x4*)(h + (size_t)row * M_ + lane * 8) = o;
}

enum { EPI_PLAIN = 0, EPI_RELU = 1, EPI_RES = 2 };
#define WAIT_VM(n) asm volatile("s_waitcnt vmcnt(%0)" ::"i"(n) : "memory")
#define WAIT_LGKM0() asm volatile("s_waitcnt lgkmcnt(0)" ::: "memory")

struct NoSide { DI void pre() {} DI void post() {} };
template <int BM, int BN, int WM, int WN, int BK, int NS, int EPI, class Side = NoSide>
DI void gemm_body(const bf16_t* __restrict__ A, int lda, const bf16_t* __restrict__ B, int ldb, bf16_t* __restrict__ C, int ldc, int K,
                  int m0, int n0, char* smem, Side side = Side()) {
  constexpr int NW = WM * WN;
  constexpr int WTM = BM / WM, WTN = BN / WN, TM = WTM / 32, TN = WTN / 32;
  constexpr int AROW = BK * 2, BROW = BN * 2;
  constexpr int A_BYTES = BM * AROW, B_BYTES = BK * BROW, BUF = A_BYTES + B_BYTES;
  constexpr int A_INSTR = A_BYTES / 1024, B_INSTR = B_BYTES / 1024;
  static_assert(A_INSTR % NW == 0 && B_INSTR % NW == 0, "staging split");
  constexpr int PER = (A_INSTR + B_INSTR) / NW;
  constexpr int ACH = AROW / 16;
  const int tid = threadIdx.x, lane = tid & 63, w = __builtin_amdgcn_readfirstlane(tid >> 6);
  const int wm = w / WN, wn = w % WN, r32 = lane & 31, hi = lane >> 5;

  const bf16_t* asrc[A_INSTR / NW];
  const bf16_t* bsrc[B_INSTR / NW];
#pragma unroll
  for (int i = 0; i < A_INSTR / NW; ++i) {
    const int piece = w + i * NW, u = piece * 64 + lane, r = u / ACH, cc = u % ACH;
    const int ch = cc ^ (BK == 64 ? ((r >> 1) & 7) : ((r >> 2) & 3));
    asrc[i] = A + (size_t)(m0 + r) * lda + ch * 8;
  }
  constexpr int UPR = BN / 8;
#pragma unroll
  for (int i = 0; i < B_INSTR / NW; ++i) {
    const int piece = w + i * NW, u = piece * 64 + lane, r = u / UPR, uu = u % UPR;
    const int c = (uu >> 2) ^ (BN >= 128 ? (r & 3) : ((r >> 1) & 1));
    bsrc[i] = B + (size_t)r * ldb + n0 + (c * 4 + (uu & 3)) * 8;
  }
  auto stage = [&](int t, int buf) NOPK {
    char* base = smem + buf * BUF;
#pragma unroll
    for (int i = 0; i < A_INSTR / NW; ++i) glds16(asrc[i] + t * BK, base + (w + i * NW) * 1024);
#pragma unroll
    for (int i = 0; i < B_INSTR / NW; ++i) glds16(bsrc[i] + (size_t)t * BK * ldb, base + A_BYTES + (w + i * NW) * 1024);
  };

  f32x16 acc[TM][TN];
#pragma unroll
  for (int i = 0; i < TM; ++i)
#pragma unroll
    for (int j = 0; j < TN; ++j)
#pragma unroll
      for (int r = 0; r < 16; ++r) acc[i][j][r] = 0.f;
#pragma unroll
  for (int i = 0; i < TM; ++i)
#pragma unroll
    for (int j = 0; j < TN; ++j) PIN(acc[i][j]);

  const int asw = BK == 64 ? ((lane >> 1) & 7) : ((lane >> 2) & 3);
  const int a_off = (wm * WTM + r32) * AROW;
  const int q = (lane & 15) >> 2, p = lane & 3, chalf = (lane >> 4) & 1;
  const int bsw = BN >= 128 ? q : (q >> 1);
  const int b_row = (8 * hi + q) * BROW + (chalf * 16 + 4 * p) * 2;
  const int nt = K / BK;
#pragma unroll
  for (int t = 0; t < NS - 1; ++t) stage(t, t);
  side.pre();
  int buf = 0;
  for (int t = 0; t < nt; ++t) {
    if (t + NS - 2 < nt) WAIT_VM(PER * (NS - 2)); else WAIT_VM(0);
    asm volatile("s_waitcnt lgkmcnt(0)\n\ts_barrier" ::: "memory");
    if (t + NS - 1 < nt) { int nb = buf + NS - 1; if (nb >= NS) nb -= NS; stage(t + NS - 1, nb); }
    const char* As = smem + buf * BUF;
    const char* Bs = As + A_BYTES;
#pragma unroll
    for (int s = 0; s < BK / 16; ++s) {
      bf16x8 a[TM], b[TN];
#pragma unroll
      for (int i = 0; i < TM; ++i) a[i] = *(const bf16x8*)(As + a_off + i * 32 * AROW + (((2 * s + hi) ^ asw) << 4));
#pragma unroll
      for (int j = 0; j < TN; ++j) {
        const int cj = (wn * WTN) / 32 + j;
        const char* pb = Bs + b_row + ((cj ^ bsw) << 6) + (16 * s) * BROW;
        b[j] = tr2(pb, pb + 4 * BROW);
      }
#pragma unroll
      for (int i = 0; i < TM; ++i)
#pragma unroll
        for (int j = 0; j < TN; ++j) acc[i][j] = mfma32(b[j], a[i], acc[i][j]);
    }
    ++buf; if (buf == NS) buf = 0;
  }
  SYNC();
  constexpr int ERS = WTN * 2 + 16;
  char* ep = smem + w * (WTM * ERS);
#pragma unroll
  for (int i = 0; i < TM; ++i)
#pragma unroll
    for (int j = 0; j < TN; ++j)
#pragma unroll
      for (int gq = 0; gq < 4; ++gq) {
        float v0 = acc[i][j][4 * gq], v1 = acc[i][j][4 * gq + 1], v2 = acc[i][j][4 * gq + 2], v3 = acc[i][j][4 * gq + 3];
        if (EPI == EPI_RELU) { v0 = fmaxf(v0, 0.f); v1 = fmaxf(v1, 0.f); v2 = fmaxf(v2, 0.f); v3 = fmaxf(v3, 0.f); }
        u32x2 o = {pk_bf16(v0, v1), pk_bf16(v2, v3)};
        *(u32x2*)(ep + (i * 32 + r32) * ERS + (j * 32 + 8 * gq + 4 * hi) * 2) = o;
      }
  constexpr int LPR = WTN / 8, RPI = 64 / LPR;
  const int er = lane / LPR, ec = lane % LPR;
#pragma unroll
  for (int it = 0; it < WTM / RPI; ++it) {
    const int r = it * RPI + er;
    u32x4 v = *(const u32x4*)(ep + r * ERS + ec * 16);
    if (EPI == EPI_RELU) {
      const int row = m0 + wm * WTM + r, ch = (n0 + wn * WTN) / 8 + ec;
      *(u32x4*)(C + ((size_t)((row >> 5) * ldc + (ch >> 1)) * 64 + (row & 31) + 32 * (ch & 1)) * 8) = v;
    } else {
      *(u32x4*)(C + (size_t)(m0 + wm * WTM + r) * ldc + n0 + wn * WTN + ec * 8) = v;
    }
  }
  side.post();
}

template <int BM, int BN, int WM, int WN, int BK, int NS, int EPI>
constexpr int gemm_smem() {
  constexpr int a = NS * (BM * BK * 2 + BK * BN * 2), b = WM * WN * (BM / WM) * ((BN / WN) * 2 + 16);
  return a > b ? a : b;
}

template <int BM, int BN, int WM, int WN, int BK, int NS, int EPI>
NOPK __global__ __launch_bounds__(WM* WN * 64) void gemm_kernel(const bf16_t* A, const bf16_t* B, bf16_t* C, int lda, int ldb, int ldc, int K, int ntn) {
  extern __shared__ __attribute__((aligned(16))) char smem[];
  const int bm = blockIdx.x / ntn, bn = blockIdx.x % ntn;
  gemm_body<BM, BN, WM, WN, BK, NS, EPI>(A, lda, B, ldb, C, ldc, K, bm * BM, bn * BN, smem);
}

struct KvSide {
  const bf16_t* c; const bf16_t* wmod; float* mod; bf16_t* modb; const bf16_t* x; float* stats; bf16_t* xt; char* scratch;
  u32x4 xv, w0, w1; bf16_t cb;
  DI void pre() {
    const int tid = threadIdx.x, lane = tid & 63, w = tid >> 6, b = blockIdx.x;
    xv = *(const u32x4*)(x + (size_t)(b * 8 + w) * M_ + lane * 8);
    const int bb = b < 192 ? b : 0;
    cb = c[tid];
    w0 = *(const u32x4*)(wmod + (size_t)tid * 3072 + bb * 16); w1 = *(const u32x4*)(wmod + (size_t)tid * 3072 + bb * 16 + 8);
  }
  DI void post() {
    const int tid = threadIdx.x, lane = tid & 63, w = tid >> 6, b = blockIdx.x;
    {
      const int row = b * 8 + w;
      *(u32x4*)(xt + ((size_t)((row >> 5) * 32 + (lane >> 1)) * 64 + (row & 31) + 32 * (lane & 1)) * 8) = xv;
      float f[8];
#pragma unroll
      for (int j = 0; j < 4; ++j) { f[2 * j] = bf_lo(xv[j]); f[2 * j + 1] = bf_hi(xv[j]); }
      float sm = 0;
#pragma unroll
      for (int j = 0; j < 8; ++j) sm += f[j];
#pragma unroll
      for (int o = 32; o; o >>= 1) sm += shx(sm, o);
      const float mean = sm * (1.f / M_);
      float q = 0;
#pragma unroll
      for (int j = 0; j < 8; ++j) { const float d = f[j] - mean; q += d * d; }
#pragma unroll
      for (int o = 32; o; o >>= 1) q += shx(q, o);
      if (lane == 0) { f32x2 st = {mean, rsqrtf(q * (1.f / M_) + 1e-5f)}; *(f32x2*)(stats + 2 * row) = st; }
    }
    if (b < 192) {
      const float cv = bf2f(cb);
      const float sv = rbf(cv / (1.f + __expf(-cv)));
      float a[16];
#pragma unroll
      for (int j = 0; j < 4; ++j) { a[2 * j] = sv * bf_lo(w0[j]); a[2 * j + 1] = sv * bf_hi(w0[j]); a[8 + 2 * j] = sv * bf_lo(w1[j]); a[8 + 2 * j + 1] = sv * bf_hi(w1[j]); }
#pragma unroll
      for (int j = 0; j < 16; ++j)
#pragma unroll
        for (int o = 32; o; o >>= 1) a[j] += shx(a[j], o);
      SYNC();
      float* red = (float*)scratch;
      if (lane == 0) {
#pragma unroll
        for (int j = 0; j < 16; ++j) red[w * 16 + j] = a[j];
      }
      SYNC();
      if (tid < 16) {
        float t = 0;
#pragma unroll
        for (int i = 0; i < 8; ++i) t += red[i * 16 + tid];
        const int n = b * 16 + tid;
        const float r = rbf(t);
        mod[n] = r;
        if (n < 512) modb[n] = (bf16_t)(pk_bf16(r, 0.f) & 0xffff);
        else if (n < 1024) modb[n] = (bf16_t)(pk_bf16(1.f + r, 0.f) & 0xffff);
        else if (n >= 1536 && n < 2048) modb[n - 512] = (bf16_t)(pk_bf16(r, 0.f) & 0xffff);
        else if (n >= 2048 && n < 2560) modb[n - 512] = (bf16_t)(pk_bf16(1.f + r, 0.f) & 0xffff);
      }
    }
  }
};
template <int BM, int BN, int WM, int WN, int BK, int NS>
NOPK __global__ __launch_bounds__(WM* WN * 64) void kv_kernel(const bf16_t* ckv, const bf16_t* wk, const bf16_t* wv, bf16_t* k, bf16_t* v,
                                                          const bf16_t* c, const bf16_t* wmod, float* mod, bf16_t* modb, const bf16_t* x, float* stats, bf16_t* xt) {
  extern __shared__ __attribute__((aligned(16))) char smem[];
  constexpr int NTN = HD_ / BN, NTM = S_ / BM, PER = NTN * NTM;
  static_assert(2 * PER == 256 && WM * WN == 8, "side jobs assume 256 blocks of 512 threads");
  const int nb = 2 * PER, xcd = blockIdx.x & 7, idx = blockIdx.x >> 3;
  const int lin = xcd * (nb / 8) + idx;
  const int which = lin / PER, b = lin % PER;
  const bf16_t* B = which == 0 ? wk : wv;
  bf16_t* C = which == 0 ? k : v;
  KvSide side; side.c = c; side.wmod = wmod; side.mod = mod; side.modb = modb; side.x = x; side.stats = stats; side.xt = xt; side.scratch = smem;
  gemm_body<BM, BN, WM, WN, BK, NS, EPI_PLAIN, KvSide>(ckv, M_, B, HD_, C, HD_, M_, (b % NTM) * BM, (b / NTM) * BN, smem, side);
}

template <int NKS>
NOPK __global__ __launch_bounds__(512) void gemm_rs(const bf16_t* __restrict__ At, const bf16_t* __restrict__ Bt, bf16_t* __restrict__ C,
                                                    const bf16_t* __restrict__ res, const float* __restrict__ gate) {
  extern __shared__ __attribute__((aligned(16))) char smem[];
  constexpr int KS = NKS * 8;
  constexpr int D = NKS < 8 ? NKS : 8;
  const int tid = threadIdx.x, lane = tid & 63, w = __builtin_amdgcn_readfirstlane(tid >> 6);
  const int r32 = lane & 31, hi = lane >> 5;
  const int xcd = blockIdx.x & 7, idx = blockIdx.x >> 3;
  const int mb = xcd * 4 + (idx >> 3), nb = idx & 7;
  const auto ra = __builtin_amdgcn_make_buffer_rsrc((void*)At, 0, S_ * KS * 32, 0x00020000);
  const auto rb = __builtin_amdgcn_make_buffer_rsrc((void*)Bt, 0, KS * 16 * M_ * 2, 0x00020000);
  const int vo = lane * 16;
  const int sa0 = ((2 * mb) * KS + w * NKS) * 1024, sa1 = sa0 + KS * 1024;
  const int sb0 = ((w * NKS) * 16 + 2 * nb) * 1024;
  f32x16 acc[2][2];
#pragma unroll
  for (int i = 0; i < 2; ++i)
#pragma unroll
    for (int j = 0; j < 2; ++j)
#pragma unroll
      for (int r = 0; r < 16; ++r) acc[i][j][r] = 0.f;
  PIN(acc[0][0]); PIN(acc[0][1]); PIN(acc[1][0]); PIN(acc[1][1]);
  u32x4 fa[D][2], fb[D][2];
#define RS_LOAD(SL, T) do { \
    fa[SL][0] = __builtin_amdgcn_raw_buffer_load_b128(ra, vo, sa0 + (T) * 1024, 0); \
    fb[SL][0] = __builtin_amdgcn_raw_buffer_load_b128(rb, vo, sb0 + (T) * 16384, 0); \
    fa[SL][1] = __builtin_amdgcn_raw_buffer_load_b128(ra, vo, sa1 + (T) * 1024, 0); \
    fb[SL][1] = __builtin_amdgcn_raw_buffer_load_b128(rb, vo, sb0 + (T) * 16384 + 1024, 0); } while (0)
#pragma unroll
  for (int t = 0; t < D; ++t) RS_LOAD(t, t);
  __builtin_amdgcn_sched_barrier(0);
#pragma unroll
  for (int t = 0; t < NKS; ++t) {
    const int sl = t % D;
#pragma unroll
    for (int i = 0; i < 2; ++i)
#pragma unroll
      for (int j = 0; j < 2; ++j)
        acc[i][j] = mfma32(__builtin_bit_cast(bf16x8, fb[sl][j]), __builtin_bit_cast(bf16x8, fa[sl][i]), acc[i][j]);
    __builtin_amdgcn_sched_barrier(0);
    if (t + D < NKS) RS_LOAD(sl, t + D);
    __builtin_amdgcn_sched_barrier(0);
  }
  constexpr int RS = 272;
  {
    char* pw = smem + w * (64 * RS);
#pragma unroll
    for (int i = 0; i < 2; ++i)
#pragma unroll
      for (int j = 0; j < 2; ++j)
#pragma unroll
        for (int gq = 0; gq < 4; ++gq) {
          f32x4 v = {acc[i][j][4 * gq], acc[i][j][4 * gq + 1], acc[i][j][4 * gq + 2], acc[i][j][4 * gq + 3]};
          *(f32x4*)(pw + (i * 32 + r32) * RS + (j * 32 + 8 * gq + 4 * hi) * 4) = v;
        }
  }
  SYNC();
  {
    const int m0 = mb * 64, n0 = nb * 64;
    const int m = tid >> 3, nn = (tid & 7) * 8;
    f32x4 s0 = {0, 0, 0, 0}, s1 = {0, 0, 0, 0};
#pragma unroll
    for (int pp = 0; pp < 8; ++pp) {
      const char* src = smem + pp * (64 * RS) + m * RS + nn * 4;
      s0 += *(const f32x4*)src; s1 += *(const f32x4*)(src + 16);
    }
    const u32x4 rr = *(const u32x4*)(res + (size_t)(m0 + m) * M_ + n0 + nn);
    const f32x4 g0 = *(const f32x4*)(gate + n0 + nn), g1 = *(const f32x4*)(gate + n0 + nn + 4);
    u32x4 o;
    o[0] = pk_bf16(bf_lo(rr[0]) + rbf(g0[0] * rbf(s0[0])), bf_hi(rr[0]) + rbf(g0[1] * rbf(s0[1])));
    o[1] = pk_bf16(bf_lo(rr[1]) + rbf(g0[2] * rbf(s0[2])), bf_hi(rr[1]) + rbf(g0[3] * rbf(s0[3])));
    o[2] = pk_bf16(bf_lo(rr[2]) + rbf(g1[0] * rbf(s1[0])), bf_hi(rr[2]) + rbf(g1[1] * rbf(s1[1])));
    o[3] = pk_bf16(bf_lo(rr[3]) + rbf(g1[2] * rbf(s1[2])), bf_hi(rr[3]) + rbf(g1[3] * rbf(s1[3])));
    *(u32x4*)(C + (size_t)(m0 + m) * M_ + n0 + nn) = o;
  }
}

constexpr float ATT_C2 = 0.08838834764831845f * 1.4426950408889634f;
#define DSR128(dst, addr, off) asm volatile("ds_read_b128 %0, %1 offset:%2" : "=v"(dst) : "v"(addr), "i"(off))
#define DSRTR(dst, addr, off) asm volatile("ds_read_b64_tr_b16 %0, %1 offset:%2" : "=v"(dst) : "v"(addr), "i"(off))
#define SB0() __builtin_amdgcn_sched_barrier(0)
DI void glds_s(const char* sbase, unsigned voff, unsigned lds_addr) {
  asm volatile("s_mov_b32 m0, %2\n\ts_nop 0\n\tglobal_load_lds_dwordx4 %0, %1" ::"v"(voff), "s"(sbase), "s"(lds_addr) : "memory");
}
#define K_READ2(F, S0) do { const unsigned a0_ = kaddr ^ ((S0) << 5), a1_ = kaddr ^ (((S0) + 1) << 5); \
    DSR128(F[0], a0_, 0); DSR128(F[1], a0_, 8192); DSR128(F[2], a1_, 0); DSR128(F[3], a1_, 8192); } while (0)
#define WAIT4(N, F) asm volatile("s_waitcnt lgkmcnt(" #N ")" : "+v"(F[0]), "+v"(F[1]), "+v"(F[2]), "+v"(F[3]))
#define K_MMA2(F, S0) do { \
    _Pragma("unroll") for (int s_ = 0; s_ < 2; ++s_) { s0 = mfma32(F[2 * s_], qr[S0 + s_], s0); s1 = mfma32(F[2 * s_ + 1], qr[S0 + s_], s1); } } while (0)
#define ATT_QK(HOOK) do { \
  bf16x8 fa[4], fb[4]; \
  K_READ2(fa, 0); \
  K_READ2(fb, 2); \
  if (last) { int dq_ = dq_last; asm volatile("" : "+v"(dq_)); \
    _Pragma("unroll") for (int r = 0; r < 16; ++r) { const int c_ = (r & 3) + 8 * (r >> 2); \
      s0[r] = c_ > dq_ ? -__builtin_inff() : 0.f; s1[r] = c_ + 32 > dq_ ? -__builtin_inff() : 0.f; } \
  } else { _Pragma("unroll") for (int r = 0; r < 16; ++r) { s0[r] = 0.f; s1[r] = 0.f; } } \
  WAIT4(4, fa); SB0(); \
  K_MMA2(fa, 0); SB0(); \
  K_READ2(fa, 4); HOOK(0); \
  WAIT4(4, fb); SB0(); \
  K_MMA2(fb, 2); SB0(); \
  K_READ2(fb, 6); HOOK(1); \
  WAIT4(4, fa); SB0(); \
  K_MMA2(fa, 4); SB0(); HOOK(2); \
  WAIT4(0, fb); SB0(); \
  K_MMA2(fb, 6); SB0(); HOOK(3); } while (0)
struct VFrag { s16x4 lo[4], hi[4]; };
#define V_READ1(F, S) do { \
      DSRTR(F.lo[0], va0, (S) * 4096); DSRTR(F.hi[0], va0, (S) * 4096 + 2048); \
      DSRTR(F.lo[1], va1, (S) * 4096); DSRTR(F.hi[1], va1, (S) * 4096 + 2048); \
      DSRTR(F.lo[2], va2, (S) * 4096); DSRTR(F.hi[2], va2, (S) * 4096 + 2048); \
      DSRTR(F.lo[3], va3, (S) * 4096); DSRTR(F.hi[3], va3, (S) * 4096 + 2048); } while (0)
#define VWAIT(N, F) asm volatile("s_waitcnt lgkmcnt(" #N ")" : "+v"(F.lo[0]), "+v"(F.lo[1]), "+v"(F.lo[2]), "+v"(F.lo[3]), "+v"(F.hi[0]), "+v"(F.hi[1]), "+v"(F.hi[2]), "+v"(F.hi[3]))
#define V_MMA1(F, S) do { const bf16x8 pb_ = __builtin_bit_cast(bf16x8, pw[S]); \
      _Pragma("unroll") for (int d_ = 0; d_ < 4; ++d_) \
        o[d_] = mfma32(__builtin_shufflevector(F.lo[d_], F.hi[d_], 0, 1, 2, 3, 4, 5, 6, 7), pb_, o[d_]); } while (0)
#define V_ADDR() const unsigned va0 = vaddr, va1 = vaddr ^ 64, va2 = vaddr ^ 128, va3 = vaddr ^ 192
#define PV_TAIL(HOOK) do { \
      VWAIT(8, f0); SB0(); V_MMA1(f0, 0); SB0(); V_READ1(f0, 2); HOOK(0); \
      VWAIT(8, f1); SB0(); V_MMA1(f1, 1); SB0(); V_READ1(f1, 3); HOOK(1); \
      VWAIT(8, f0); SB0(); V_MMA1(f0, 2); SB0(); HOOK(2); \
      VWAIT(0, f1); SB0(); V_MMA1(f1, 3); SB0(); HOOK(3); } while (0)
#define ATT_SM() do { \
  float mx = s0[0]; \
  _Pragma("unroll") for (int r = 1; r < 16; ++r) mx = fmaxf(mx, s0[r]); \
  _Pragma("unroll") for (int r = 0; r < 16; ++r) mx = fmaxf(mx, s1[r]); \
  { auto rr = __builtin_amdgcn_permlane32_swap(f2u(mx), f2u(mx), false, false); \
    mx = fmaxf(u2f(rr[0]), u2f(rr[1])); } \
  const float mnew = fmaxf(m, mx); \
  const float alpha = __builtin_amdgcn_exp2f((m - mnew) * ATT_C2); \
  m = mnew; \
  const float mc = -mnew * ATT_C2; \
  float ps = 0.f; \
  _Pragma("unroll") for (int r = 0; r < 16; ++r) { s0[r] = __builtin_amdgcn_exp2f(fmaf(s0[r], ATT_C2, mc)); ps += s0[r]; } \
  _Pragma("unroll") for (int r = 0; r < 16; ++r) { s1[r] = __builtin_amdgcn_exp2f(fmaf(s1[r], ATT_C2, mc)); ps += s1[r]; } \
  l = l * alpha + ps; \
  if (any_lane(alpha != 1.f)) { \
    _Pragma("unroll") for (int d = 0; d < 4; ++d) _Pragma("unroll") for (int r = 0; r < 16; ++r) o[d][r] *= alpha; } \
  _Pragma("unroll") for (int s = 0; s < 4; ++s) _Pragma("unroll") for (int j = 0; j < 4; ++j) { \
      const int r = 8 * (s & 1) + 2 * j; \
      pw[s][j] = s < 2 ? pk_bf16(s0[r], s0[r + 1]) : pk_bf16(s1[r], s1[r + 1]); } } while (0)
#define ATT_BAR() asm volatile("s_waitcnt vmcnt(0) lgkmcnt(0)\n\ts_barrier" ::: "memory")
#define NOHOOK(i) do {} while (0)

NOPK __global__ __launch_bounds__(512) void attn_kernel(const bf16_t* __restrict__ X, const float* __restrict__ stats, const bf16_t* __restrict__ modb,
                                                   const bf16_t* __restrict__ Wq, const bf16_t* __restrict__ Kg, const bf16_t* __restrict__ Vg, bf16_t* __restrict__ ctx,
                                                   const bf16_t* __restrict__ Wo, const bf16_t* __restrict__ W2, bf16_t* __restrict__ Wot, bf16_t* __restrict__ W2t) {
  extern __shared__ __attribute__((aligned(16))) char smem[];
  const int tid = threadIdx.x, lane = tid & 63, w = __builtin_amdgcn_readfirstlane(tid >> 6), g = w >> 2, wq = w & 3;
  const int r32 = lane & 31, hi = lane >> 5;
  const int b = blockIdx.x, head = (b & 7) + 8 * (b >> 6), pair = (b >> 3) & 7;
  const unsigned lds0 = (unsigned)(uintptr_t)smem;
  const unsigned Kl = lds0 + g * 65536, Vl = Kl + 32768;
  const int srow = 4 * wq + (lane >> 4), su = lane & 15;
  const unsigned ksrc = (unsigned)(srow * HD_ + head * D_ + ((su ^ srow) << 3)) * 2u;
  const unsigned vsrc = (unsigned)(srow * HD_ + head * D_ + (((((su >> 2) ^ (srow & 3)) << 2) | (su & 3)) << 3)) * 2u;
#define STAGE_K1(tile, buf, i) glds_s((const char*)Kg + (size_t)((tile) * 64 + 16 * (i)) * (HD_ * 2), ksrc, Kl + (buf) * 16384 + wq * 1024 + (i) * 4096)
#define STAGE_V1(tile, buf, i) glds_s((const char*)Vg + (size_t)((tile) * 64 + 16 * (i)) * (HD_ * 2), vsrc, Vl + (buf) * 16384 + wq * 1024 + (i) * 4096)
  const int q4 = (lane & 15) >> 2, p4 = lane & 3, chalf = (lane >> 4) & 1;
  const unsigned kaddr0 = Kl + r32 * 256 + ((hi ^ (r32 & 15)) << 4);
  const unsigned vaddr0 = Vl + (4 * hi + q4) * 256 + (chalf * 16 + 4 * p4) * 2 + (q4 << 6);

  bf16x8 qr[8];
  char* Qs = smem + 131072;
  {
    {
      const int kr = lane >> 4;
      const unsigned wsrc = (unsigned)(kr * HD_ + head * D_ + (((((su >> 2) ^ kr) << 2) | (su & 3)) << 3)) * 2u;
#pragma unroll
      for (int i = 0; i < 16; ++i) glds_s((const char*)Wq + (size_t)((w * 16 + i) * 4) * (HD_ * 2), wsrc, lds0 + (w * 16 + i) * 1024);
    }
    const int boff = (8 * hi + q4) * 256 + (chalf * 16 + 4 * p4) * 2;
    const int xq = g == 0 ? 15 - pair : pair;
    const f32x2 stq = *(const f32x2*)(stats + 2 * (128 * xq + 32 * wq + r32));
    const float rs = stq[1], nm = -stq[0] * stq[1];
    const auto rx = __builtin_amdgcn_make_buffer_rsrc((void*)X, 0, S_ * M_ * 2, 0x00020000);
    const auto rm = __builtin_amdgcn_make_buffer_rsrc((void*)modb, 0, 4096, 0x00020000);
    const int voX = lane * 16, voM = hi * 16;
    const int soX = (4 * xq + wq) * 32768;
    f32x16 qa[4];
#pragma unroll
    for (int r = 0; r < 16; ++r) { qa[0][r] = 0.f; qa[1][r] = 0.f; qa[2][r] = 0.f; qa[3][r] = 0.f; }
    u32x4 xa[2][4], sv[2][4], cv[2][4];
#define QP_LOAD(B_, C_) do { _Pragma("unroll") for (int s = 0; s < 4; ++s) { \
      xa[B_][s] = __builtin_amdgcn_raw_buffer_load_b128(rx, voX, soX + ((C_) * 4 + s) * 1024, 0); \
      sv[B_][s] = __builtin_amdgcn_raw_buffer_load_b128(rm, voM, ((C_) * 4 + s) * 32, 0); \
      cv[B_][s] = __builtin_amdgcn_raw_buffer_load_b128(rm, voM, 1024 + ((C_) * 4 + s) * 32, 0); } } while (0)
    QP_LOAD(0, 0);
    {
#pragma unroll
      for (int i = 0; i < 3; ++i) {
        const int f = (blockIdx.x * 8 + w) + i * 2048;
        if (f < 4096 + 1792) {
          const bool first = f < 4096;
          const int ff = first ? f : f - 4096;
          const bf16_t* src = (first ? Wo : W2) + (size_t)((ff >> 4) * 16 + 8 * hi) * M_ + (ff & 15) * 32 + r32;
          unsigned short e[8];
#pragma unroll
          for (int j = 0; j < 8; ++j) e[j] = src[(size_t)j * M_];
          u32x4 pk;
#pragma unroll
          for (int j = 0; j < 4; ++j) pk[j] = (unsigned)e[2 * j] | ((unsigned)e[2 * j + 1] << 16);
          *(u32x4*)((first ? Wot : W2t) + ((size_t)ff * 64 + lane) * 8) = pk;
        }
      }
    }
    ATT_BAR();
#pragma unroll
    for (int kc = 0; kc < 8; ++kc) {
      const int cb = kc & 1;
      if (kc + 1 < 8) QP_LOAD(cb ^ 1, kc + 1);
      SB0();
#pragma unroll
      for (int s = 0; s < 4; ++s) {
        u32x4 ha;
#pragma unroll
        for (int j = 0; j < 4; ++j)
          ha[j] = pk_bf16(fmaf(fmaf(bf_lo(xa[cb][s][j]), rs, nm), bf_lo(cv[cb][s][j]), bf_lo(sv[cb][s][j])),
                          fmaf(fmaf(bf_hi(xa[cb][s][j]), rs, nm), bf_hi(cv[cb][s][j]), bf_hi(sv[cb][s][j])));
        const bf16x8 fa_ = __builtin_bit_cast(bf16x8, ha);
#pragma unroll
        for (int j = 0; j < 4; ++j) {
          const char* pb = smem + boff + (kc * 64 + 16 * s) * 256 + ((j ^ q4) << 6);
          qa[j] = mfma32(tr2(pb, pb + 1024), fa_, qa[j]);
        }
      }
      SB0();
    }
    SYNC();
    const int lr = 32 * wq + r32;
    {
      char* dst = g == 0 ? smem : Qs;
#pragma unroll
      for (int j = 0; j < 4; ++j)
#pragma unroll
        for (int gq = 0; gq < 4; ++gq) {
          const int d = 32 * j + 8 * gq + 4 * hi;
          u32x2 ov = {pk_bf16(qa[j][4 * gq], qa[j][4 * gq + 1]), pk_bf16(qa[j][4 * gq + 2], qa[j][4 * gq + 3])};
          *(u32x2*)(dst + lr * 256 + ((((d >> 3) ^ (lr & 15))) << 4) + (d & 7) * 2) = ov;
        }
    }
    SYNC();
#pragma unroll
    for (int s = 0; s < 8; ++s) qr[s] = *(const bf16x8*)(smem + lr * 256 + (((2 * s + hi) ^ (lr & 15)) << 4));
    SYNC();
  }

#pragma nounroll
  for (int pass = 0; pass < 2; ++pass) {
    const int x = pass == 0 ? 15 - pair : pair;
    const int nt = x + 1;
    const int qrow = 128 * x + 32 * wq + r32;
    f32x16 o[4], s0, s1;
    u32x4 pw[4];
    float m = -1e30f, l = 0.f;
#pragma unroll
    for (int i = 0; i < 4; ++i) STAGE_K1(g, 0, i);
    if (g == 0) {
#pragma unroll
      for (int i = 0; i < 4; ++i) STAGE_V1(0, 0, i);
    }
    if (pass == 1) {
      const int lr = 32 * wq + r32;
#pragma unroll
      for (int s = 0; s < 8; ++s) qr[s] = *(const bf16x8*)(Qs + lr * 256 + (((2 * s + hi) ^ (lr & 15)) << 4));
    }
#pragma unroll
    for (int d = 0; d < 4; ++d)
#pragma unroll
      for (int r = 0; r < 16; ++r) o[d][r] = 0.f;
    ATT_BAR();
    const int dq_last = qrow - 64 * (2 * (nt - 1) + g) - 4 * hi;
    unsigned kaddr = kaddr0, vaddr = vaddr0;
    if (g == 0) {
#pragma nounroll
      for (int it = 0; it < nt; ++it) {
        const int buf = it & 1;
        const bool last = it + 1 == nt;
        const int nx = last ? 0 : 2 * (it + 1);
#define HOOK_A(i) do { STAGE_K1(nx, buf ^ 1, i); STAGE_V1(nx, buf ^ 1, i); } while (0)
        ATT_QK(HOOK_A);
        VFrag f0, f1;
        V_ADDR();
        V_READ1(f0, 0); V_READ1(f1, 1);
        SB0();
        ATT_SM();
        SB0();
        PV_TAIL(NOHOOK);
        kaddr ^= 16384; vaddr ^= 16384;
        ATT_BAR();
      }
    } else {
      const bool skip_last = wq < 2;
#pragma nounroll
      for (int it = 0; it < nt; ++it) {
        const int buf = it & 1;
        const bool last = it + 1 == nt;
        const int nx = last ? 0 : 2 * (it + 1) + 1;
#define HOOK_BK(i) STAGE_K1(nx, buf ^ 1, i)
#define HOOK_BV(i) STAGE_V1(2 * it + 1, buf, i)
        if (it > 0) {
          VFrag f0, f1;
          V_ADDR();
          V_READ1(f0, 0); V_READ1(f1, 1);
          PV_TAIL(HOOK_BK);
          vaddr ^= 16384;
        } else {
          HOOK_BK(0); HOOK_BK(1); HOOK_BK(2); HOOK_BK(3);
        }
        if (!(last && skip_last)) {
          ATT_QK(HOOK_BV);
          ATT_SM();
        } else {
          HOOK_BV(0); HOOK_BV(1); HOOK_BV(2); HOOK_BV(3);
        }
        kaddr ^= 16384;
        ATT_BAR();
      }
      if (!skip_last) {
        VFrag f0, f1;
        V_ADDR();
        V_READ1(f0, 0); V_READ1(f1, 1);
        PV_TAIL(NOHOOK);
      }
    }
    ATT_BAR();
    l += shx(l, 32);
    float* xo = (float*)(smem + w * 8192);
    float* sp = (float*)(smem + 65536 + w * 512);
#pragma unroll
    for (int dd = 0; dd < 2; ++dd)
#pragma unroll
      for (int r = 0; r < 16; ++r) xo[(dd * 16 + r) * 64 + lane] = g == 0 ? o[2 + dd][r] : o[dd][r];
    sp[lane] = m; sp[64 + lane] = l;
    SYNC();
    {
      const float* po = (const float*)(smem + (w ^ 4) * 8192);
      const float* pst = (const float*)(smem + 65536 + (w ^ 4) * 512);
      const float mo = pst[lane], lo = pst[64 + lane];
      const float mt = fmaxf(m, mo);
      const float a1 = __builtin_amdgcn_exp2f((m - mt) * ATT_C2), a2 = __builtin_amdgcn_exp2f((mo - mt) * ATT_C2);
      const float inv = 1.f / (l * a1 + lo * a2);
      const float f1 = a1 * inv, f2 = a2 * inv;
      bf16_t* obase = ctx + ((size_t)((qrow >> 5) * 256 + head * 8 + g * 4) * 64 + r32) * 8 + 4 * hi;
#pragma unroll
      for (int dd = 0; dd < 2; ++dd)
#pragma unroll
        for (int gq = 0; gq < 4; ++gq) {
          float v[4];
#pragma unroll
          for (int e = 0; e < 4; ++e) {
            const int r = 4 * gq + e;
            const float own = g == 0 ? o[dd][r] : o[2 + dd][r];
            v[e] = own * f1 + po[(dd * 16 + r) * 64 + lane] * f2;
          }
          u32x2 ov = {pk_bf16(v[0], v[1]), pk_bf16(v[2], v[3])};
          *(u32x2*)(obase + ((dd * 2 + (gq >> 1)) * 64 + 32 * (gq & 1)) * 8) = ov;
        }
    }
    SYNC();
  }
}

extern "C" void kernel_launch(void* const* d_in, const int* in_sizes, int n_in,
                              void* d_out, int out_size, void* d_ws, size_t ws_size,
                              hipStream_t stream) {
  const bf16_t* x = (const bf16_t*)d_in[0];
  const bf16_t* ckv = (const bf16_t*)d_in[1];
  const bf16_t* wq = (const bf16_t*)d_in[2];
  const bf16_t* wuk = (const bf16_t*)d_in[3];
  const bf16_t* wuv = (const bf16_t*)d_in[4];
  const bf16_t* wo = (const bf16_t*)d_in[5];
  const bf16_t* w1 = (const bf16_t*)d_in[7];
  const bf16_t* w2 = (const bf16_t*)d_in[8];
  const bf16_t* c = (const bf16_t*)d_in[9];
  const bf16_t* wmod = (const bf16_t*)d_in[10];
  bf16_t* out = (bf16_t*)d_out;

  char* ws = (char*)d_ws;
  float* mod = (float*)ws;
  bf16_t* h1 = (bf16_t*)(ws + (1 << 20));
  bf16_t* x1 = (bf16_t*)(ws + (4 << 20));
  bf16_t* h2 = (bf16_t*)(ws + (7 << 20));
  bf16_t* hid = (bf16_t*)(ws + (10 << 20));
  bf16_t* q = (bf16_t*)(ws + (32 << 20));
  bf16_t* k = (bf16_t*)(ws + (48 << 20));
  bf16_t* v = (bf16_t*)(ws + (64 << 20));
  bf16_t* ctx = (bf16_t*)(ws + (80 << 20));
  bf16_t* wot = (bf16_t*)(ws + (96 << 20));
  bf16_t* w2t = (bf16_t*)(ws + (100 << 20));

  bf16_t* modb = (bf16_t*)(ws + (32 << 10));
  float* stats = (float*)(ws + (64 << 10));
  {
    constexpr int BM = 256, BN = 256, WM = 2, WN = 4, BK = 32, NS = 4, SH = gemm_smem<BM, BN, WM, WN, BK, NS, 0>();
    (void)hipFuncSetAttribute((const void*)kv_kernel<BM, BN, WM, WN, BK, NS>, hipFuncAttributeMaxDynamicSharedMemorySize, SH);
    kv_kernel<BM, BN, WM, WN, BK, NS><<<256, WM * WN * 64, SH, stream>>>(ckv, wuk, wuv, k, v, c, wmod, mod, modb, x, stats, h1);
  }
  (void)hipFuncSetAttribute((const void*)attn_kernel, hipFuncAttributeMaxDynamicSharedMemorySize, 163840);
  attn_kernel<<<256, 512, 163840, stream>>>(h1, stats, modb, wq, k, v, ctx, wo, w2, wot, w2t);
  constexpr int RS_SH = 8 * 64 * 272;
  (void)hipFuncSetAttribute((const void*)gemm_rs<32>, hipFuncAttributeMaxDynamicSharedMemorySize, RS_SH);
  (void)hipFuncSetAttribute((const void*)gemm_rs<14>, hipFuncAttributeMaxDynamicSharedMemorySize, RS_SH);
  gemm_rs<32><<<256, 512, RS_SH, stream>>>(ctx, wot, x1, x, mod + 2 * 512);
  ln_kernel<<<S_ / 4, 256, 0, stream>>>(x1, mod + 3 * 512, mod + 4 * 512, h2);
  {
    constexpr int BM = 128, BN = 128, WM = 2, WN = 4, BK = 64, NS = 4, SH = gemm_smem<BM, BN, WM, WN, BK, NS, 0>();
    (void)hipFuncSetAttribute((const void*)gemm_kernel<BM, BN, WM, WN, BK, NS, EPI_RELU>, hipFuncAttributeMaxDynamicSharedMemorySize, SH);
    gemm_kernel<BM, BN, WM, WN, BK, NS, EPI_RELU><<<(S_ / BM) * (FF_ / BN), WM * WN * 64, SH, stream>>>(h2, w1, hid, M_, FF_, FF_ / 16, M_, FF_ / BN);
  }
  gemm_rs<14><<<256, 512, RS_SH, stream>>>(hid, w2t, out, x1, mod + 5 * 512);
}
```

```cpp
#include <hip/hip_runtime.h>
#include <hip/hip_bf16.h>
#include <stdint.h>

typedef unsigned short bf16_t;
typedef short s16x4 __attribute__((ext_vector_type(4)));
typedef short bf16x8 __attribute__((ext_vector_type(8)));
typedef float f32x16 __attribute__((ext_vector_type(16)));
typedef float f32x4 __attribute__((ext_vector_type(4)));
typedef float f32x2 __attribute__((ext_vector_type(2)));
typedef unsigned u32x4 __attribute__((ext_vector_type(4)));
typedef unsigned u32x2 __attribute__((ext_vector_type(2)));
typedef __bf16 bfx2 __attribute__((ext_vector_type(2)));
typedef __attribute__((address_space(3))) s16x4* lds_s16x4_ptr;
typedef __attribute__((address_space(3))) unsigned* lds_u32_ptr;

#if defined(__HIP_DEVICE_COMPILE__)
#define NOPK __attribute__((target("no-packed-fp32-ops")))
#else
#define NOPK
#endif
#define DI NOPK __device__ __forceinline__

constexpr int S_ = 2048, M_ = 512, H_ = 32, D_ = 128, HD_ = 4096, FF_ = 1792;

DI float u2f(unsigned u) { return __builtin_bit_cast(float, u); }
DI unsigned f2u(float f) { return __builtin_bit_cast(unsigned, f); }
#define SYNC() asm volatile("s_waitcnt vmcnt(0) lgkmcnt(0)\n\ts_barrier" ::: "memory")
DI float shx(float v, int o) {
  const int lane = __builtin_amdgcn_mbcnt_hi(~0u, __builtin_amdgcn_mbcnt_lo(~0u, 0u));
  return u2f((unsigned)__builtin_amdgcn_ds_bpermute((lane ^ o) << 2, (int)f2u(v)));
}
DI bool any_lane(bool p) { return __builtin_amdgcn_ballot_w64(p) != 0ull; }
DI unsigned pk_bf16(float lo, float hi) {
  f32x2 v = {lo, hi};
  bfx2 r = __builtin_convertvector(v, bfx2);
  return __builtin_bit_cast(unsigned, r);
}
DI float bf_lo(unsigned u) { return u2f(u << 16); }
DI float bf_hi(unsigned u) { return u2f(u & 0xffff0000u); }
DI float bf2f(bf16_t v) { return u2f(((unsigned)v) << 16); }
DI float rbf(float x) { return bf_lo(pk_bf16(x, 0.f)); }
DI f32x16 mfma32(bf16x8 a, bf16x8 b, f32x16 c) { return __builtin_amdgcn_mfma_f32_32x32x16_bf16(a, b, c, 0, 0, 0); }
#define PIN(x) asm volatile("" : "+v"(x))
DI bf16x8 tr2(const char* p0, const char* p1) {
  s16x4 a = __builtin_amdgcn_ds_read_tr16_b64_v4i16((lds_s16x4_ptr)(p0));
  s16x4 b = __builtin_amdgcn_ds_read_tr16_b64_v4i16((lds_s16x4_ptr)(p1));
  return __builtin_shufflevector(a, b, 0, 1, 2, 3, 4, 5, 6, 7);
}
DI void glds16(const void* g, char* l) {
  __builtin_amdgcn_global_load_lds((const unsigned*)g, (lds_u32_ptr)l, 16, 0, 0);
}

NOPK __global__ __launch_bounds__(256) void mod_kernel(const bf16_t* __restrict__ c, const bf16_t* __restrict__ wmod, float* __restrict__ mod) {
  __shared__ float red[32][64];
  const int tid = threadIdx.x, cg = tid & 7, rg = tid >> 3, n0 = blockIdx.x * 64 + cg * 8;
  float acc[8] = {0, 0, 0, 0, 0, 0, 0, 0};
#pragma unroll
  for (int i = 0; i < 16; ++i) {
    const int k = rg + 32 * i;
    float cv = bf2f(c[k]);
    float s = rbf(cv / (1.f + __expf(-cv)));
    u32x4 w = *(const u32x4*)(wmod + (size_t)k * 3072 + n0);
#pragma unroll
    for (int j = 0; j < 4; ++j) { acc[2 * j] += s * bf_lo(w[j]); acc[2 * j + 1] += s * bf_hi(w[j]); }
  }
#pragma unroll
  for (int j = 0; j < 8; ++j) red[rg][cg * 8 + j] = acc[j];
  SYNC();
  if (tid < 64) {
    float s = 0;
#pragma unroll
    for (int i = 0; i < 32; ++i) s += red[i][tid];
    mod[blockIdx.x * 64 + tid] = rbf(s);
  }
}

NOPK __global__ __launch_bounds__(256) void ln_kernel(const bf16_t* __restrict__ x, const float* __restrict__ shift, const float* __restrict__ scale, bf16_t* __restrict__ h) {
  const int lane = threadIdx.x & 63, row = 256 * (blockIdx.x & 7) + 4 * (blockIdx.x >> 3) + (threadIdx.x >> 6);
  u32x4 v = *(const u32x4*)(x + (size_t)row * M_ + lane * 8);
  float f[8];
#pragma unroll
  for (int j = 0; j < 4; ++j) { f[2 * j] = bf_lo(v[j]); f[2 * j + 1] = bf_hi(v[j]); }
  float s = 0;
#pragma unroll
  for (int j = 0; j < 8; ++j) s += f[j];
#pragma unroll
  for (int o = 32; o; o >>= 1) s += shx(s, o);
  const float mean = s * (1.f / M_);
  float q = 0;
#pragma unroll
  for (int j = 0; j < 8; ++j) { f[j] -= mean; q += f[j] * f[j]; }
#pragma unroll
  for (int o = 32; o; o >>= 1) q += shx(q, o);
  const float rstd = rsqrtf(q * (1.f / M_) + 1e-5f);
  u32x4 o;
#pragma unroll
  for (int j = 0; j < 4; ++j) {
    const int n = lane * 8 + 2 * j;
    float a = f[2 * j] * rstd * rbf(1.f + scale[n]) + shift[n];
    float b = f[2 * j + 1] * rstd * rbf(1.f + scale[n + 1]) + shift[n + 1];
    o[j] = pk_bf16(a, b);
  }
  *(u32x4*)(h + (size_t)row * M_ + lane * 8) = o;
}

enum { EPI_PLAIN = 0, EPI_RELU = 1, EPI_RES = 2 };
#define WAIT_VM(n) asm volatile("s_waitcnt vmcnt(%0)" ::"i"(n) : "memory")
#define WAIT_LGKM0() asm volatile("s_waitcnt lgkmcnt(0)" ::: "memory")

struct NoSide { DI void pre() {} DI void post() {} };
template <int BM, int BN, int WM, int WN, int BK, int NS, int EPI, class Side = NoSide>
DI void gemm_body(const bf16_t* __restrict__ A, int lda, const bf16_t* __restrict__ B, int ldb, bf16_t* __restrict__ C, int ldc, int K,
                  int m0, int n0, char* smem, Side side = Side()) {
  constexpr int NW = WM * WN;
  constexpr int WTM = BM / WM, WTN = BN / WN, TM = WTM / 32, TN = WTN / 32;
  constexpr int AROW = BK * 2, BROW = BN * 2;
  constexpr int A_BYTES = BM * AROW, B_BYTES = BK * BROW, BUF = A_BYTES + B_BYTES;
  constexpr int A_INSTR = A_BYTES / 1024, B_INSTR = B_BYTES / 1024;
  static_assert(A_INSTR % NW == 0 && B_INSTR % NW == 0, "staging split");
  constexpr int PER = (A_INSTR + B_INSTR) / NW;
  constexpr int ACH = AROW / 16;
  const int tid = threadIdx.x, lane = tid & 63, w = __builtin_amdgcn_readfirstlane(tid >> 6);
  const int wm = w / WN, wn = w % WN, r32 = lane & 31, hi = lane >> 5;

  const bf16_t* asrc[A_INSTR / NW];
  const bf16_t* bsrc[B_INSTR / NW];
#pragma unroll
  for (int i = 0; i < A_INSTR / NW; ++i) {
    const int piece = w + i * NW, u = piece * 64 + lane, r = u / ACH, cc = u % ACH;
    const int ch = cc ^ (BK == 64 ? ((r >> 1) & 7) : ((r >> 2) & 3));
    asrc[i] = A + (size_t)(m0 + r) * lda + ch * 8;
  }
  constexpr int UPR = BN / 8;
#pragma unroll
  for (int i = 0; i < B_INSTR / NW; ++i) {
    const int piece = w + i * NW, u = piece * 64 + lane, r = u / UPR, uu = u % UPR;
    const int c = (uu >> 2) ^ (BN >= 128 ? (r & 3) : ((r >> 1) & 1));
    bsrc[i] = B + (size_t)r * ldb + n0 + (c * 4 + (uu & 3)) * 8;
  }
  auto stage = [&](int t, int buf) NOPK {
    char* base = smem + buf * BUF;
#pragma unroll
    for (int i = 0; i < A_INSTR / NW; ++i) glds16(asrc[i] + t * BK, base + (w + i * NW) * 1024);
#pragma unroll
    for (int i = 0; i < B_INSTR / NW; ++i) glds16(bsrc[i] + (size_t)t * BK * ldb, base + A_BYTES + (w + i * NW) * 1024);
  };

  f32x16 acc[TM][TN];
#pragma unroll
  for (int i = 0; i < TM; ++i)
#pragma unroll
    for (int j = 0; j < TN; ++j)
#pragma unroll
      for (int r = 0; r < 16; ++r) acc[i][j][r] = 0.f;
#pragma unroll
  for (int i = 0; i < TM; ++i)
#pragma unroll
    for (int j = 0; j < TN; ++j) PIN(acc[i][j]);

  const int asw = BK == 64 ? ((lane >> 1) & 7) : ((lane >> 2) & 3);
  const int a_off = (wm * WTM + r32) * AROW;
  const int q = (lane & 15) >> 2, p = lane & 3, chalf = (lane >> 4) & 1;
  const int bsw = BN >= 128 ? q : (q >> 1);
  const int b_row = (8 * hi + q) * BROW + (chalf * 16 + 4 * p) * 2;
  const int nt = K / BK;
#pragma unroll
  for (int t = 0; t < NS - 1; ++t) stage(t, t);
  side.pre();
#define PP_BAR() asm volatile("s_barrier" ::: "memory")
  static_assert(WM == 2, "ping-pong needs two wave groups");
  constexpr int KSN = BK / 16;
  bf16x8 fa[KSN][TM], fb[KSN][TN];
  WAIT_VM(PER * (NS - 2));
  PP_BAR();
  if (wm == 1) PP_BAR();
  int buf = 0;
  for (int t = 0; t < nt; ++t) {
    {
      const char* As = smem + buf * BUF;
      const char* Bs = As + A_BYTES;
#pragma unroll
      for (int s = 0; s < KSN; ++s) {
#pragma unroll
        for (int i = 0; i < TM; ++i) fa[s][i] = *(const bf16x8*)(As + a_off + i * 32 * AROW + (((2 * s + hi) ^ asw) << 4));
#pragma unroll
        for (int j = 0; j < TN; ++j) {
          const int cj = (wn * WTN) / 32 + j;
          const char* pb = Bs + b_row + ((cj ^ bsw) << 6) + (16 * s) * BROW;
          const unsigned pa_ = (unsigned)(uintptr_t)pb;
          s16x4 lo_, hi_;
          asm volatile("ds_read_b64_tr_b16 %0, %1" : "=v"(lo_) : "v"(pa_));
          asm volatile("ds_read_b64_tr_b16 %0, %1 offset:%2" : "=v"(hi_) : "v"(pa_), "i"(4 * BROW));
          fb[s][j] = __builtin_shufflevector(lo_, hi_, 0, 1, 2, 3, 4, 5, 6, 7);
        }
      }
      if (t + NS - 1 < nt) { int nb = buf + NS - 1; if (nb >= NS) nb -= NS; stage(t + NS - 1, nb); }
    }
    if (wm == 1) { if (t + NS - 1 < nt) WAIT_VM(PER * (NS - 2)); else WAIT_VM(0); }
    asm volatile("s_waitcnt lgkmcnt(0)\n\ts_barrier" ::: "memory");
#pragma unroll
    for (int s = 0; s < KSN; ++s) {
#pragma unroll
      for (int i = 0; i < TM; ++i) asm volatile("" : "+v"(fa[s][i]));
#pragma unroll
      for (int j = 0; j < TN; ++j) asm volatile("" : "+v"(fb[s][j]));
    }
    __builtin_amdgcn_sched_barrier(0);
    __builtin_amdgcn_s_setprio(1);
#pragma unroll
    for (int s = 0; s < KSN; ++s)
#pragma unroll
      for (int i = 0; i < TM; ++i)
#pragma unroll
        for (int j = 0; j < TN; ++j) acc[i][j] = mfma32(fb[s][j], fa[s][i], acc[i][j]);
    __builtin_amdgcn_s_setprio(0);
    __builtin_amdgcn_sched_barrier(0);
    if (wm == 0) { if (t + NS - 1 < nt) WAIT_VM(PER * (NS - 2)); else WAIT_VM(0); }
    PP_BAR();
    ++buf; if (buf == NS) buf = 0;
  }
  if (wm == 0) PP_BAR();
  SYNC();
  constexpr int ERS = WTN * 2 + 16;
  char* ep = smem + w * (WTM * ERS);
#pragma unroll
  for (int i = 0; i < TM; ++i)
#pragma unroll
    for (int j = 0; j < TN; ++j)
#pragma unroll
      for (int gq = 0; gq < 4; ++gq) {
        float v0 = acc[i][j][4 * gq], v1 = acc[i][j][4 * gq + 1], v2 = acc[i][j][4 * gq + 2], v3 = acc[i][j][4 * gq + 3];
        if (EPI == EPI_RELU) { v0 = fmaxf(v0, 0.f); v1 = fmaxf(v1, 0.f); v2 = fmaxf(v2, 0.f); v3 = fmaxf(v3, 0.f); }
        u32x2 o = {pk_bf16(v0, v1), pk_bf16(v2, v3)};
        *(u32x2*)(ep + (i * 32 + r32) * ERS + (j * 32 + 8 * gq + 4 * hi) * 2) = o;
      }
  constexpr int LPR = WTN / 8, RPI = 64 / LPR;
  const int er = lane / LPR, ec = lane % LPR;
#pragma unroll
  for (int it = 0; it < WTM / RPI; ++it) {
    const int r = it * RPI + er;
    u32x4 v = *(const u32x4*)(ep + r * ERS + ec * 16);
    if (EPI == EPI_RELU) {
      const int row = m0 + wm * WTM + r, ch = (n0 + wn * WTN) / 8 + ec;
      *(u32x4*)(C + ((size_t)((row >> 5) * ldc + (ch >> 1)) * 64 + (row & 31) + 32 * (ch & 1)) * 8) = v;
    } else {
      *(u32x4*)(C + (size_t)(m0 + wm * WTM + r) * ldc + n0 + wn * WTN + ec * 8) = v;
    }
  }
  side.post();
}

template <int BM, int BN, int WM, int WN, int BK, int NS, int EPI>
constexpr int gemm_smem() {
  constexpr int a = NS * (BM * BK * 2 + BK * BN * 2), b = WM * WN * (BM / WM) * ((BN / WN) * 2 + 16);
  return a > b ? a : b;
}

template <int BM, int BN, int WM, int WN, int BK, int NS, int EPI>
NOPK __global__ __launch_bounds__(WM* WN * 64) void gemm_kernel(const bf16_t* A, const bf16_t* B, bf16_t* C, int lda, int ldb, int ldc, int K, int ntn) {
  extern __shared__ __attribute__((aligned(16))) char smem[];
  const int idx = blockIdx.x >> 3, bm = 2 * (blockIdx.x & 7) + idx / ntn, bn = idx % ntn;
  gemm_body<BM, BN, WM, WN, BK, NS, EPI>(A, lda, B, ldb, C, ldc, K, bm * BM, bn * BN, smem);
}

struct KvSide {
  const bf16_t* c; const bf16_t* wmod; float* mod; bf16_t* modb; const bf16_t* x; float* stats; bf16_t* xt; char* scratch;
  u32x4 xv, w0, w1; bf16_t cb;
  DI void pre() {
    const int tid = threadIdx.x, lane = tid & 63, w = tid >> 6, b = blockIdx.x;
    xv = *(const u32x4*)(x + (size_t)(b * 8 + w) * M_ + lane * 8);
    const int bb = b < 192 ? b : 0;
    cb = c[tid];
    w0 = *(const u32x4*)(wmod + (size_t)tid * 3072 + bb * 16); w1 = *(const u32x4*)(wmod + (size_t)tid * 3072 + bb * 16 + 8);
  }
  DI void post() {
    const int tid = threadIdx.x, lane = tid & 63, w = tid >> 6, b = blockIdx.x;
    {
      const int row = b * 8 + w;
      *(u32x4*)(xt + ((size_t)((row >> 5) * 32 + (lane >> 1)) * 64 + (row & 31) + 32 * (lane & 1)) * 8) = xv;
      float f[8];
#pragma unroll
      for (int j = 0; j < 4; ++j) { f[2 * j] = bf_lo(xv[j]); f[2 * j + 1] = bf_hi(xv[j]); }
      float sm = 0;
#pragma unroll
      for (int j = 0; j < 8; ++j) sm += f[j];
#pragma unroll
      for (int o = 32; o; o >>= 1) sm += shx(sm, o);
      const float mean = sm * (1.f / M_);
      float q = 0;
#pragma unroll
      for (int j = 0; j < 8; ++j) { const float d = f[j] - mean; q += d * d; }
#pragma unroll
      for (int o = 32; o; o >>= 1) q += shx(q, o);
      if (lane == 0) { f32x2 st = {mean, rsqrtf(q * (1.f / M_) + 1e-5f)}; *(f32x2*)(stats + 2 * row) = st; }
    }
    if (b < 192) {
      const float cv = bf2f(cb);
      const float sv = rbf(cv / (1.f + __expf(-cv)));
      float a[16];
#pragma unroll
      for (int j = 0; j < 4; ++j) { a[2 * j] = sv * bf_lo(w0[j]); a[2 * j + 1] = sv * bf_hi(w0[j]); a[8 + 2 * j] = sv * bf_lo(w1[j]); a[8 + 2 * j + 1] = sv * bf_hi(w1[j]); }
      float b8[8], b4[4], b2[2], b1;
      {
        const bool up = lane & 32;
#pragma unroll
        for (int j = 0; j < 8; ++j) { const float keep = up ? a[8 + j] : a[j], give = up ? a[j] : a[8 + j]; b8[j] = keep + shx(give, 32); }
      }
      {
        const bool up = lane & 16;
#pragma unroll
        for (int j = 0; j < 4; ++j) { const float keep = up ? b8[4 + j] : b8[j], give = up ? b8[j] : b8[4 + j]; b4[j] = keep + shx(give, 16); }
      }
      {
        const bool up = lane & 8;
#pragma unroll
        for (int j = 0; j < 2; ++j) { const float keep = up ? b4[2 + j] : b4[j], give = up ? b4[j] : b4[2 + j]; b2[j] = keep + shx(give, 8); }
      }
      {
        const bool up = lane & 4;
        const float keep = up ? b2[1] : b2[0], give = up ? b2[0] : b2[1];
        b1 = keep + shx(give, 4);
      }
      b1 += shx(b1, 2);
      b1 += shx(b1, 1);
      SYNC();
      float* red = (float*)scratch;
      if ((lane & 3) == 0) red[w * 16 + (lane >> 2)] = b1;
      SYNC();
      if (tid < 16) {
        float t = 0;
#pragma unroll
        for (int i = 0; i < 8; ++i) t += red[i * 16 + tid];
        const int n = b * 16 + tid;
        const float r = rbf(t);
        mod[n] = r;
        float* mf = (float*)modb;
        if (n < 512) mf[n] = r;
        else if (n < 1024) mf[n] = rbf(1.f + r);
        else if (n >= 1536 && n < 2048) mf[n - 512] = r;
        else if (n >= 2048 && n < 2560) mf[n - 512] = rbf(1.f + r);
      }
    }
  }
};
template <int BM, int BN, int WM, int WN, int BK, int NS>
NOPK __global__ __launch_bounds__(WM* WN * 64) void kv_kernel(const bf16_t* ckv, const bf16_t* wk, const bf16_t* wv, bf16_t* k, bf16_t* v,
                                                          const bf16_t* c, const bf16_t* wmod, float* mod, bf16_t* modb, const bf16_t* x, float* stats, bf16_t* xt) {
  extern __shared__ __attribute__((aligned(16))) char smem[];
  constexpr int NTN = HD_ / BN, NTM = S_ / BM, PER = NTN * NTM;
  static_assert(2 * PER == 256 && WM * WN == 8, "side jobs assume 256 blocks of 512 threads");
  const int xcd = blockIdx.x & 7, idx = blockIdx.x >> 3;
  const int which = idx >> 4, b = (2 * xcd + ((idx >> 3) & 1)) * NTM + (idx & 7);
  const bf16_t* B = which == 0 ? wk : wv;
  bf16_t* C = which == 0 ? k : v;
  KvSide side; side.c = c; side.wmod = wmod; side.mod = mod; side.modb = modb; side.x = x; side.stats = stats; side.xt = xt; side.scratch = smem;
  gemm_body<BM, BN, WM, WN, BK, NS, EPI_PLAIN, KvSide>(ckv, M_, B, HD_, C, HD_, M_, (b % NTM) * BM, (b / NTM) * BN, smem, side);
}

template <int NKS>
NOPK __global__ __launch_bounds__(512) void gemm_rs(const bf16_t* __restrict__ At, const bf16_t* __restrict__ Bt, bf16_t* __restrict__ C,
                                                    const bf16_t* __restrict__ res, const float* __restrict__ gate) {
  extern __shared__ __attribute__((aligned(16))) char smem[];
  constexpr int KS = NKS * 8;
  constexpr int D = NKS < 8 ? NKS : 8;
  const int tid = threadIdx.x, lane = tid & 63, w = __builtin_amdgcn_readfirstlane(tid >> 6);
  const int r32 = lane & 31, hi = lane >> 5;
  const int xcd = blockIdx.x & 7, idx = blockIdx.x >> 3;
  const int mb = xcd * 4 + (idx >> 3), nb = idx & 7;
  const auto ra = __builtin_amdgcn_make_buffer_rsrc((void*)At, 0, S_ * KS * 32, 0x00020000);
  const auto rb = __builtin_amdgcn_make_buffer_rsrc((void*)Bt, 0, KS * 16 * M_ * 2, 0x00020000);
  const int vo = lane * 16;
  const int sa0 = ((2 * mb) * KS + w * NKS) * 1024, sa1 = sa0 + KS * 1024;
  const int sb0 = ((w * NKS) * 16 + 2 * nb) * 1024;
  f32x16 acc[2][2];
#pragma unroll
  for (int i = 0; i < 2; ++i)
#pragma unroll
    for (int j = 0; j < 2; ++j)
#pragma unroll
      for (int r = 0; r < 16; ++r) acc[i][j][r] = 0.f;
  PIN(acc[0][0]); PIN(acc[0][1]); PIN(acc[1][0]); PIN(acc[1][1]);
  u32x4 fa[D][2], fb[D][2];
#define RS_LOAD(SL, T) do { \
    fa[SL][0] = __builtin_amdgcn_raw_buffer_load_b128(ra, vo, sa0 + (T) * 1024, 0); \
    fb[SL][0] = __builtin_amdgcn_raw_buffer_load_b128(rb, vo, sb0 + (T) * 16384, 0); \
    fa[SL][1] = __builtin_amdgcn_raw_buffer_load_b128(ra, vo, sa1 + (T) * 1024, 0); \
    fb[SL][1] = __builtin_amdgcn_raw_buffer_load_b128(rb, vo, sb0 + (T) * 16384 + 1024, 0); } while (0)
#pragma unroll
  for (int t = 0; t < D; ++t) RS_LOAD(t, t);
  __builtin_amdgcn_sched_barrier(0);
#pragma unroll
  for (int t = 0; t < NKS; ++t) {
    const int sl = t % D;
#pragma unroll
    for (int i = 0; i < 2; ++i)
#pragma unroll
      for (int j = 0; j < 2; ++j)
        acc[i][j] = mfma32(__builtin_bit_cast(bf16x8, fb[sl][j]), __builtin_bit_cast(bf16x8, fa[sl][i]), acc[i][j]);
    __builtin_amdgcn_sched_barrier(0);
    if (t + D < NKS) RS_LOAD(sl, t + D);
    __builtin_amdgcn_sched_barrier(0);
  }
  constexpr int RS = 272;
  {
    char* pw = smem + w * (64 * RS);
#pragma unroll
    for (int i = 0; i < 2; ++i)
#pragma unroll
      for (int j = 0; j < 2; ++j)
#pragma unroll
        for (int gq = 0; gq < 4; ++gq) {
          f32x4 v = {acc[i][j][4 * gq], acc[i][j][4 * gq + 1], acc[i][j][4 * gq + 2], acc[i][j][4 * gq + 3]};
          *(f32x4*)(pw + (i * 32 + r32) * RS + (j * 32 + 8 * gq + 4 * hi) * 4) = v;
        }
  }
  SYNC();
  {
    const int m0 = mb * 64, n0 = nb * 64;
    const int m = tid >> 3, nn = (tid & 7) * 8;
    f32x4 s0 = {0, 0, 0, 0}, s1 = {0, 0, 0, 0};
#pragma unroll
    for (int pp = 0; pp < 8; ++pp) {
      const char* src = smem + pp * (64 * RS) + m * RS + nn * 4;
      s0 += *(const f32x4*)src; s1 += *(const f32x4*)(src + 16);
    }
    const u32x4 rr = *(const u32x4*)(res + (size_t)(m0 + m) * M_ + n0 + nn);
    const f32x4 g0 = *(const f32x4*)(gate + n0 + nn), g1 = *(const f32x4*)(gate + n0 + nn + 4);
    u32x4 o;
    o[0] = pk_bf16(bf_lo(rr[0]) + rbf(g0[0] * rbf(s0[0])), bf_hi(rr[0]) + rbf(g0[1] * rbf(s0[1])));
    o[1] = pk_bf16(bf_lo(rr[1]) + rbf(g0[2] * rbf(s0[2])), bf_hi(rr[1]) + rbf(g0[3] * rbf(s0[3])));
    o[2] = pk_bf16(bf_lo(rr[2]) + rbf(g1[0] * rbf(s1[0])), bf_hi(rr[2]) + rbf(g1[1] * rbf(s1[1])));
    o[3] = pk_bf16(bf_lo(rr[3]) + rbf(g1[2] * rbf(s1[2])), bf_hi(rr[3]) + rbf(g1[3] * rbf(s1[3])));
    *(u32x4*)(C + (size_t)(m0 + m) * M_ + n0 + nn) = o;
  }
}

constexpr float ATT_C2 = 0.08838834764831845f * 1.4426950408889634f;
#define DSR128(dst, addr, off) asm volatile("ds_read_b128 %0, %1 offset:%2" : "=v"(dst) : "v"(addr), "i"(off))
#define DSRTR(dst, addr, off) asm volatile("ds_read_b64_tr_b16 %0, %1 offset:%2" : "=v"(dst) : "v"(addr), "i"(off))
#define SB0() __builtin_amdgcn_sched_barrier(0)
DI void glds_s(const char* sbase, unsigned voff, unsigned lds_addr) {
  asm volatile("s_mov_b32 m0, %2\n\ts_nop 0\n\tglobal_load_lds_dwordx4 %0, %1" ::"v"(voff), "s"(sbase), "s"(lds_addr) : "memory");
}
#define K_READ2(F, S0) do { const unsigned a0_ = kaddr ^ ((S0) << 5), a1_ = kaddr ^ (((S0) + 1) << 5); \
    DSR128(F[0], a0_, 0); DSR128(F[1], a0_, 8192); DSR128(F[2], a1_, 0); DSR128(F[3], a1_, 8192); } while (0)
#define WAIT4(N, F) asm volatile("s_waitcnt lgkmcnt(" #N ")" : "+v"(F[0]), "+v"(F[1]), "+v"(F[2]), "+v"(F[3]))
#define K_MMA2(F, S0) do { \
    _Pragma("unroll") for (int s_ = 0; s_ < 2; ++s_) { s0 = mfma32(F[2 * s_], qr[S0 + s_], s0); s1 = mfma32(F[2 * s_ + 1], qr[S0 + s_], s1); } } while (0)
#define ATT_QK(HOOK) do { \
  bf16x8 fa[4], fb[4]; \
  K_READ2(fa, 0); \
  K_READ2(fb, 2); \
  if (last) { int dq_ = dq_last; asm volatile("" : "+v"(dq_)); \
    _Pragma("unroll") for (int r = 0; r < 16; ++r) { const int c_ = (r & 3) + 8 * (r >> 2); \
      s0[r] = c_ > dq_ ? -__builtin_inff() : 0.f; s1[r] = c_ + 32 > dq_ ? -__builtin_inff() : 0.f; } \
  } else { _Pragma("unroll") for (int r = 0; r < 16; ++r) { s0[r] = 0.f; s1[r] = 0.f; } } \
  WAIT4(4, fa); SB0(); \
  K_MMA2(fa, 0); SB0(); \
  K_READ2(fa, 4); HOOK(0); \
  WAIT4(4, fb); SB0(); \
  K_MMA2(fb, 2); SB0(); \
  K_READ2(fb, 6); HOOK(1); \
  WAIT4(4, fa); SB0(); \
  K_MMA2(fa, 4); SB0(); HOOK(2); \
  WAIT4(0, fb); SB0(); \
  K_MMA2(fb, 6); SB0(); HOOK(3); } while (0)
struct VFrag { s16x4 lo[4], hi[4]; };
#define V_READ1(F, S) do { \
      DSRTR(F.lo[0], va0, (S) * 4096); DSRTR(F.hi[0], va0, (S) * 4096 + 2048); \
      DSRTR(F.lo[1], va1, (S) * 4096); DSRTR(F.hi[1], va1, (S) * 4096 + 2048); \
      DSRTR(F.lo[2], va2, (S) * 4096); DSRTR(F.hi[2], va2, (S) * 4096 + 2048); \
      DSRTR(F.lo[3], va3, (S) * 4096); DSRTR(F.hi[3], va3, (S) * 4096 + 2048); } while (0)
#define VWAIT(N, F) asm volatile("s_waitcnt lgkmcnt(" #N ")" : "+v"(F.lo[0]), "+v"(F.lo[1]), "+v"(F.lo[2]), "+v"(F.lo[3]), "+v"(F.hi[0]), "+v"(F.hi[1]), "+v"(F.hi[2]), "+v"(F.hi[3]))
#define V_MMA1(F, S) do { const bf16x8 pb_ = __builtin_bit_cast(bf16x8, pw[S]); \
      _Pragma("unroll") for (int d_ = 0; d_ < 4; ++d_) \
        o[d_] = mfma32(__builtin_shufflevector(F.lo[d_], F.hi[d_], 0, 1, 2, 3, 4, 5, 6, 7), pb_, o[d_]); } while (0)
#define V_ADDR() const unsigned va0 = vaddr, va1 = vaddr ^ 64, va2 = vaddr ^ 128, va3 = vaddr ^ 192
#define PV_TAIL(HOOK) do { \
      VWAIT(8, f0); SB0(); V_MMA1(f0, 0); SB0(); V_READ1(f0, 2); HOOK(0); \
      VWAIT(8, f1); SB0(); V_MMA1(f1, 1); SB0(); V_READ1(f1, 3); HOOK(1); \
      VWAIT(8, f0); SB0(); V_MMA1(f0, 2); SB0(); HOOK(2); \
      VWAIT(0, f1); SB0(); V_MMA1(f1, 3); SB0(); HOOK(3); } while (0)
#define ATT_SM() do { \
  float mx = s0[0]; \
  _Pragma("unroll") for (int r = 1; r < 16; ++r) mx = fmaxf(mx, s0[r]); \
  _Pragma("unroll") for (int r = 0; r < 16; ++r) mx = fmaxf(mx, s1[r]); \
  { auto rr = __builtin_amdgcn_permlane32_swap(f2u(mx), f2u(mx), false, false); \
    mx = fmaxf(u2f(rr[0]), u2f(rr[1])); } \
    \
  if (any_lane((mx - m) * ATT_C2 > 8.f)) { \
    const float mnew = fmaxf(m, mx); \
    const float alpha = __builtin_amdgcn_exp2f((m - mnew) * ATT_C2); \
    m = mnew; l *= alpha; \
    _Pragma("unroll") for (int d = 0; d < 4; ++d) _Pragma("unroll") for (int r = 0; r < 16; ++r) o[d][r] *= alpha; } \
  const float mc = -m * ATT_C2; \
  float ps = 0.f; \
  _Pragma("unroll") for (int r = 0; r < 16; ++r) { s0[r] = __builtin_amdgcn_exp2f(fmaf(s0[r], ATT_C2, mc)); ps += s0[r]; } \
  _Pragma("unroll") for (int r = 0; r < 16; ++r) { s1[r] = __builtin_amdgcn_exp2f(fmaf(s1[r], ATT_C2, mc)); ps += s1[r]; } \
  l += ps; \
  _Pragma("unroll") for (int s = 0; s < 4; ++s) _Pragma("unroll") for (int j = 0; j < 4; ++j) { \
      const int r = 8 * (s & 1) + 2 * j; \
      pw[s][j] = s < 2 ? pk_bf16(s0[r], s0[r + 1]) : pk_bf16(s1[r], s1[r + 1]); } } while (0)
#define ATT_BAR() asm volatile("s_waitcnt vmcnt(0) lgkmcnt(0)\n\ts_barrier" ::: "memory")
#define NOHOOK(i) do {} while (0)

NOPK __global__ __launch_bounds__(512) void attn_kernel(const bf16_t* __restrict__ X, const float* __restrict__ stats, const bf16_t* __restrict__ modb,
                                                   const bf16_t* __restrict__ Wq, const bf16_t* __restrict__ Kg, const bf16_t* __restrict__ Vg, bf16_t* __restrict__ ctx,
                                                   const bf16_t* __restrict__ Wo, const bf16_t* __restrict__ W2, bf16_t* __restrict__ Wot, bf16_t* __restrict__ W2t) {
  extern __shared__ __attribute__((aligned(16))) char smem[];
  const int tid = threadIdx.x, lane = tid & 63, w = __builtin_amdgcn_readfirstlane(tid >> 6), g = w >> 2, wq = w & 3;
  const int r32 = lane & 31, hi = lane >> 5;
  const int b = blockIdx.x, head = 4 * (b & 7) + (b >> 6), pair = (b >> 3) & 7;
  const unsigned lds0 = (unsigned)(uintptr_t)smem;
  const unsigned Kl = lds0 + g * 65536, Vl = Kl + 32768;
  const int srow = 4 * wq + (lane >> 4), su = lane & 15;
  const unsigned ksrc = (unsigned)(srow * HD_ + head * D_ + ((su ^ srow) << 3)) * 2u;
  const unsigned vsrc = (unsigned)(srow * HD_ + head * D_ + (((((su >> 2) ^ (srow & 3)) << 2) | (su & 3)) << 3)) * 2u;
#define STAGE_K1(tile, buf, i) glds_s((const char*)Kg + (size_t)((tile) * 64 + 16 * (i)) * (HD_ * 2), ksrc, Kl + (buf) * 16384 + wq * 1024 + (i) * 4096)
#define STAGE_V1(tile, buf, i) glds_s((const char*)Vg + (size_t)((tile) * 64 + 16 * (i)) * (HD_ * 2), vsrc, Vl + (buf) * 16384 + wq * 1024 + (i) * 4096)
  const int q4 = (lane & 15) >> 2, p4 = lane & 3, chalf = (lane >> 4) & 1;
  const unsigned kaddr0 = Kl + r32 * 256 + ((hi ^ (r32 & 15)) << 4);
  const unsigned vaddr0 = Vl + (4 * hi + q4) * 256 + (chalf * 16 + 4 * p4) * 2 + (q4 << 6);

  bf16x8 qr[8];
  char* Qs = smem + 131072;
  {
    {
      const int kr = lane >> 4;
      const unsigned wsrc = (unsigned)(kr * HD_ + head * D_ + (((((su >> 2) ^ kr) << 2) | (su & 3)) << 3)) * 2u;
#pragma unroll
      for (int i = 0; i < 16; ++i) glds_s((const char*)Wq + (size_t)((w * 16 + i) * 4) * (HD_ * 2), wsrc, lds0 + (w * 16 + i) * 1024);
    }
    const int boff = (8 * hi + q4) * 256 + (chalf * 16 + 4 * p4) * 2;
    const int xq = g == 0 ? 15 - pair : pair;
    const f32x2 stq = *(const f32x2*)(stats + 2 * (128 * xq + 32 * wq + r32));
    const float rs = stq[1], nm = -stq[0] * stq[1];
    const auto rx = __builtin_amdgcn_make_buffer_rsrc((void*)X, 0, S_ * M_ * 2, 0x00020000);
    const int voX = lane * 16;
    const int soX = (4 * xq + wq) * 32768;
    if (w < 4) glds_s((const char*)modb + w * 1024, (unsigned)voX, lds0 + 131072 + w * 1024);
    f32x16 qa[4];
#pragma unroll
    for (int r = 0; r < 16; ++r) { qa[0][r] = 0.f; qa[1][r] = 0.f; qa[2][r] = 0.f; qa[3][r] = 0.f; }
    u32x4 xa[8];
#pragma unroll
    for (int t = 0; t < 8; ++t) xa[t] = __builtin_amdgcn_raw_buffer_load_b128(rx, voX, soX + t * 1024, 0);
    ATT_BAR();
    unsigned short re[3][8];
#pragma unroll
    for (int i = 0; i < 3; ++i) {
      const int f0 = (blockIdx.x * 8 + w) + i * 2048, f = f0 < 4096 + 1792 ? f0 : 0;
      const bool first = f < 4096;
      const int ff = first ? f : f - 4096;
      const bf16_t* src = (first ? Wo : W2) + (size_t)((ff >> 4) * 16 + 8 * hi) * M_ + (ff & 15) * 32 + r32;
#pragma unroll
      for (int j = 0; j < 8; ++j) re[i][j] = src[(size_t)j * M_];
    }
    const unsigned wb_lo = lds0 + boff + (q4 << 6), wb_hi = wb_lo + 65536;
    s16x4 wl[2][4], wh[2][4];
#define W_READ(B_, KS_) do { const unsigned a_ = (KS_) < 16 ? wb_lo : wb_hi; \
      { DSRTR(wl[B_][0], a_, ((KS_) & 15) * 4096); DSRTR(wh[B_][0], a_, ((KS_) & 15) * 4096 + 1024); } \
      { const unsigned aj_ = a_ ^ 64; DSRTR(wl[B_][1], aj_, ((KS_) & 15) * 4096); DSRTR(wh[B_][1], aj_, ((KS_) & 15) * 4096 + 1024); } \
      { const unsigned aj_ = a_ ^ 128; DSRTR(wl[B_][2], aj_, ((KS_) & 15) * 4096); DSRTR(wh[B_][2], aj_, ((KS_) & 15) * 4096 + 1024); } \
      { const unsigned aj_ = a_ ^ 192; DSRTR(wl[B_][3], aj_, ((KS_) & 15) * 4096); DSRTR(wh[B_][3], aj_, ((KS_) & 15) * 4096 + 1024); } } while (0)
    const unsigned cfa = lds0 + 131072 + hi * 32;
    u32x4 cf[2][4];
#define C_READ(B_, KS_) do { DSR128(cf[B_][0], cfa, (KS_) * 64); DSR128(cf[B_][1], cfa, (KS_) * 64 + 16); \
      DSR128(cf[B_][2], cfa, 2048 + (KS_) * 64); DSR128(cf[B_][3], cfa, 2048 + (KS_) * 64 + 16); } while (0)
#define W_WAIT(N, B_) asm volatile("s_waitcnt lgkmcnt(" #N ")" : "+v"(wl[B_][0]), "+v"(wl[B_][1]), "+v"(wl[B_][2]), "+v"(wl[B_][3]), "+v"(wh[B_][0]), "+v"(wh[B_][1]), "+v"(wh[B_][2]), "+v"(wh[B_][3]), \
      "+v"(cf[B_][0]), "+v"(cf[B_][1]), "+v"(cf[B_][2]), "+v"(cf[B_][3]))
#define QP_STEP(KS_) do { \
        if ((KS_) + 1 < 32) { C_READ(((KS_) & 1) ^ 1, ((KS_) + 1) & 31); W_READ(((KS_) & 1) ^ 1, ((KS_) + 1) & 31); } \
        if ((KS_) + 1 < 32) W_WAIT(12, (KS_) & 1); else W_WAIT(0, (KS_) & 1); \
        u32x4 ha; \
        _Pragma("unroll") for (int j = 0; j < 4; ++j) \
          ha[j] = pk_bf16(fmaf(fmaf(bf_lo(xa[(KS_) & 7][j]), rs, nm), u2f(cf[(KS_) & 1][2 + (j >> 1)][(2 * j) & 3]), u2f(cf[(KS_) & 1][j >> 1][(2 * j) & 3])), \
                          fmaf(fmaf(bf_hi(xa[(KS_) & 7][j]), rs, nm), u2f(cf[(KS_) & 1][2 + (j >> 1)][(2 * j + 1) & 3]), u2f(cf[(KS_) & 1][j >> 1][(2 * j + 1) & 3]))); \
        const bf16x8 fa_ = __builtin_bit_cast(bf16x8, ha); \
        SB0(); \
        if ((KS_) + 8 < 32) xa[(KS_) & 7] = __builtin_amdgcn_raw_buffer_load_b128(rx, voX, soX + ((KS_) + 8) * 1024, 0); \
        _Pragma("unroll") for (int j = 0; j < 4; ++j) qa[j] = mfma32(__builtin_shufflevector(wl[(KS_) & 1][j], wh[(KS_) & 1][j], 0, 1, 2, 3, 4, 5, 6, 7), fa_, qa[j]); \
        SB0(); } while (0)
#define QP_4(K0) do { QP_STEP(K0); QP_STEP(K0 + 1); QP_STEP(K0 + 2); QP_STEP(K0 + 3); } while (0)
    C_READ(0, 0); W_READ(0, 0);
    QP_4(0); QP_4(4); QP_4(8); QP_4(12); QP_4(16); QP_4(20); QP_4(24); QP_4(28);
#pragma unroll
    for (int i = 0; i < 3; ++i) {
      const int f = (blockIdx.x * 8 + w) + i * 2048;
      if (f < 4096 + 1792) {
        const bool first = f < 4096;
        const int ff = first ? f : f - 4096;
        u32x4 pk;
#pragma unroll
        for (int j = 0; j < 4; ++j) pk[j] = (unsigned)re[i][2 * j] | ((unsigned)re[i][2 * j + 1] << 16);
        *(u32x4*)((first ? Wot : W2t) + ((size_t)ff * 64 + lane) * 8) = pk;
      }
    }
    asm volatile("s_waitcnt lgkmcnt(0)\n\ts_barrier" ::: "memory");
    const int lr = 32 * wq + r32;
    {
      char* dst = g == 0 ? smem : Qs;
#pragma unroll
      for (int j = 0; j < 4; ++j)
#pragma unroll
        for (int gq = 0; gq < 4; ++gq) {
          const int d = 32 * j + 8 * gq + 4 * hi;
          u32x2 ov = {pk_bf16(qa[j][4 * gq], qa[j][4 * gq + 1]), pk_bf16(qa[j][4 * gq + 2], qa[j][4 * gq + 3])};
          *(u32x2*)(dst + lr * 256 + ((((d >> 3) ^ (lr & 15))) << 4) + (d & 7) * 2) = ov;
        }
    }
    SYNC();
#pragma unroll
    for (int s = 0; s < 8; ++s) qr[s] = *(const bf16x8*)(smem + lr * 256 + (((2 * s + hi) ^ (lr & 15)) << 4));
    SYNC();
  }

  unsigned kaddr = kaddr0, vaddr = vaddr0;
  int p0 = 0;
#pragma unroll
  for (int i = 0; i < 4; ++i) STAGE_K1(g, 0, i);
  if (g == 0) {
#pragma unroll
    for (int i = 0; i < 4; ++i) STAGE_V1(0, 0, i);
  }
#pragma nounroll
  for (int pass = 0; pass < 2; ++pass) {
    const int x = pass == 0 ? 15 - pair : pair;
    const int nt = x + 1;
    const int qrow = 128 * x + 32 * wq + r32;
    f32x16 o[4], s0, s1;
    u32x4 pw[4];
    float m = -1e30f, l = 0.f;
    if (pass == 1) {
      const int lr = 32 * wq + r32;
#pragma unroll
      for (int s = 0; s < 8; ++s) qr[s] = *(const bf16x8*)(Qs + lr * 256 + (((2 * s + hi) ^ (lr & 15)) << 4));
    }
#pragma unroll
    for (int d = 0; d < 4; ++d)
#pragma unroll
      for (int r = 0; r < 16; ++r) o[d][r] = 0.f;
    if (pass == 0) ATT_BAR();
    const int dq_last = qrow - 64 * (2 * (nt - 1) + g) - 4 * hi;
    if (g == 0) {
#pragma nounroll
      for (int it = 0; it < nt; ++it) {
        const int buf = (it + p0) & 1;
        const bool last = it + 1 == nt;
        const int nx = last ? 0 : 2 * (it + 1);
#define HOOK_A(i) do { STAGE_K1(nx, buf ^ 1, i); STAGE_V1(nx, buf ^ 1, i); } while (0)
        ATT_QK(HOOK_A);
        VFrag f0, f1;
        V_ADDR();
        V_READ1(f0, 0); V_READ1(f1, 1);
#define BAL_MODE 0
#pragma unroll
        for (int i = 0; i < 4; ++i) {
          if (BAL_MODE <= 1) glds_s((const char*)Kg + (size_t)((nx + 1) * 64 + 16 * i) * (HD_ * 2), ksrc, Kl + 65536 + (buf ^ 1) * 16384 + wq * 1024 + i * 4096);
          if (BAL_MODE == 0) glds_s((const char*)Vg + (size_t)((2 * it + 1) * 64 + 16 * i) * (HD_ * 2), vsrc, Vl + 65536 + buf * 16384 + wq * 1024 + i * 4096);
        }
        SB0();
        ATT_SM();
        SB0();
        PV_TAIL(NOHOOK);
        kaddr ^= 16384; vaddr ^= 16384;
        ATT_BAR();
      }
    } else {
      const bool skip_last = wq < 2;
#pragma nounroll
      for (int it = 0; it < nt; ++it) {
        const int buf = (it + p0) & 1;
        const bool last = it + 1 == nt;
        if (it > 0) {
          VFrag f0, f1;
          V_ADDR();
          V_READ1(f0, 0); V_READ1(f1, 1);
          PV_TAIL(NOHOOK);
          vaddr ^= 16384;
        }
        const int nxb = last ? 1 : 2 * (it + 1) + 1;
#define HOOK_BV(i) do { if (BAL_MODE >= 1) STAGE_V1(2 * it + 1, buf, i); if (BAL_MODE == 2) STAGE_K1(nxb, buf ^ 1, i); } while (0)
        if (!(last && skip_last)) {
          ATT_QK(HOOK_BV);
          ATT_SM();
        } else {
          HOOK_BV(0); HOOK_BV(1); HOOK_BV(2); HOOK_BV(3);
        }
        kaddr ^= 16384;
        ATT_BAR();
      }
      if (!skip_last) {
        VFrag f0, f1;
        V_ADDR();
        V_READ1(f0, 0); V_READ1(f1, 1);
        PV_TAIL(NOHOOK);
      }
      vaddr ^= 16384;
    }
    ATT_BAR();
    const int pl = (nt - 1 + p0) & 1;
    l += shx(l, 32);
    float* xo = (float*)(smem + g * 65536 + (wq >> 1) * 32768 + pl * 16384 + (wq & 1) * 8192);
    float* sp = (float*)(smem + 65536 + 32768 + (pl ^ 1) * 16384 + w * 512);
#pragma unroll
    for (int dd = 0; dd < 2; ++dd)
#pragma unroll
      for (int r = 0; r < 16; ++r) xo[(dd * 16 + r) * 64 + lane] = g == 0 ? o[2 + dd][r] : o[dd][r];
    sp[lane] = m; sp[64 + lane] = l;
    SYNC();
    {
      const float* po = (const float*)(smem + (g ^ 1) * 65536 + (wq >> 1) * 32768 + pl * 16384 + (wq & 1) * 8192);
      const float* pst = (const float*)(smem + 65536 + 32768 + (pl ^ 1) * 16384 + (w ^ 4) * 512);
      const float mo = pst[lane], lo = pst[64 + lane];
      const float mt = fmaxf(m, mo);
      const float a1 = __builtin_amdgcn_exp2f((m - mt) * ATT_C2), a2 = __builtin_amdgcn_exp2f((mo - mt) * ATT_C2);
      const float inv = 1.f / (l * a1 + lo * a2);
      const float f1 = a1 * inv, f2 = a2 * inv;
      bf16_t* obase = ctx + ((size_t)((qrow >> 5) * 256 + head * 8 + g * 4) * 64 + r32) * 8 + 4 * hi;
#pragma unroll
      for (int dd = 0; dd < 2; ++dd)
#pragma unroll
        for (int gq = 0; gq < 4; ++gq) {
          float v[4];
#pragma unroll
          for (int e = 0; e < 4; ++e) {
            const int r = 4 * gq + e;
            const float own = g == 0 ? o[dd][r] : o[2 + dd][r];
            v[e] = own * f1 + po[(dd * 16 + r) * 64 + lane] * f2;
          }
          u32x2 ov = {pk_bf16(v[0], v[1]), pk_bf16(v[2], v[3])};
          *(u32x2*)(obase + ((dd * 2 + (gq >> 1)) * 64 + 32 * (gq & 1)) * 8) = ov;
        }
    }
    asm volatile("s_waitcnt lgkmcnt(0)\n\ts_barrier" ::: "memory");
    p0 = (p0 + nt) & 1;
  }
}

extern "C" void kernel_launch(void* const* d_in, const int* in_sizes, int n_in,
                              void* d_out, int out_size, void* d_ws, size_t ws_size,
                              hipStream_t stream) {
  const bf16_t* x = (const bf16_t*)d_in[0];
  const bf16_t* ckv = (const bf16_t*)d_in[1];
  const bf16_t* wq = (const bf16_t*)d_in[2];
  const bf16_t* wuk = (const bf16_t*)d_in[3];
  const bf16_t* wuv = (const bf16_t*)d_in[4];
  const bf16_t* wo = (const bf16_t*)d_in[5];
  const bf16_t* w1 = (const bf16_t*)d_in[7];
  const bf16_t* w2 = (const bf16_t*)d_in[8];
  const bf16_t* c = (const bf16_t*)d_in[9];
  const bf16_t* wmod = (const bf16_t*)d_in[10];
  bf16_t* out = (bf16_t*)d_out;

  char* ws = (char*)d_ws;
  float* mod = (float*)ws;
  bf16_t* h1 = (bf16_t*)(ws + (1 << 20));
  bf16_t* x1 = (bf16_t*)(ws + (4 << 20));
  bf16_t* h2 = (bf16_t*)(ws + (7 << 20));
  bf16_t* hid = (bf16_t*)(ws + (10 << 20));
  bf16_t* q = (bf16_t*)(ws + (32 << 20));
  bf16_t* k = (bf16_t*)(ws + (48 << 20));
  bf16_t* v = (bf16_t*)(ws + (64 << 20));
  bf16_t* ctx = (bf16_t*)(ws + (80 << 20));
  bf16_t* wot = (bf16_t*)(ws + (96 << 20));
  bf16_t* w2t = (bf16_t*)(ws + (100 << 20));

  bf16_t* modb = (bf16_t*)(ws + (32 << 10));
  float* stats = (float*)(ws + (64 << 10));
  {
    constexpr int BM = 256, BN = 256, WM = 2, WN = 4, BK = 32, NS = 4, SH = gemm_smem<BM, BN, WM, WN, BK, NS, 0>();
    (void)hipFuncSetAttribute((const void*)kv_kernel<BM, BN, WM, WN, BK, NS>, hipFuncAttributeMaxDynamicSharedMemorySize, SH);
    kv_kernel<BM, BN, WM, WN, BK, NS><<<256, WM * WN * 64, SH, stream>>>(ckv, wuk, wuv, k, v, c, wmod, mod, modb, x, stats, h1);
  }
  (void)hipFuncSetAttribute((const void*)attn_kernel, hipFuncAttributeMaxDynamicSharedMemorySize, 163840);
  attn_kernel<<<256, 512, 163840, stream>>>(h1, stats, modb, wq, k, v, ctx, wo, w2, wot, w2t);
  constexpr int RS_SH = 8 * 64 * 272;
  (void)hipFuncSetAttribute((const void*)gemm_rs<32>, hipFuncAttributeMaxDynamicSharedMemorySize, RS_SH);
  (void)hipFuncSetAttribute((const void*)gemm_rs<14>, hipFuncAttributeMaxDynamicSharedMemorySize, RS_SH);
  gemm_rs<32><<<256, 512, RS_SH, stream>>>(ctx, wot, x1, x, mod + 2 * 512);
  ln_kernel<<<S_ / 4, 256, 0, stream>>>(x1, mod + 3 * 512, mod + 4 * 512, h2);
  {
    constexpr int BM = 128, BN = 128, WM = 2, WN = 4, BK = 64, NS = 4, SH = gemm_smem<BM, BN, WM, WN, BK, NS, 0>();
    (void)hipFuncSetAttribute((const void*)gemm_kernel<BM, BN, WM, WN, BK, NS, EPI_RELU>, hipFuncAttributeMaxDynamicSharedMemorySize, SH);
    gemm_kernel<BM, BN, WM, WN, BK, NS, EPI_RELU><<<(S_ / BM) * (FF_ / BN), WM * WN * 64, SH, stream>>>(h2, w1, hid, M_, FF_, FF_ / 16, M_, FF_ / BN);
  }
  gemm_rs<14><<<256, 512, RS_SH, stream>>>(hid, w2t, out, x1, mod + 5 * 512);
}
```

```cpp
#include <hip/hip_runtime.h>
#include <hip/hip_bf16.h>
#include <stdint.h>

typedef unsigned short bf16_t;
typedef short s16x4 __attribute__((ext_vector_type(4)));
typedef short bf16x8 __attribute__((ext_vector_type(8)));
typedef float f32x16 __attribute__((ext_vector_type(16)));
typedef float f32x4 __attribute__((ext_vector_type(4)));
typedef float f32x2 __attribute__((ext_vector_type(2)));
typedef unsigned u32x4 __attribute__((ext_vector_type(4)));
typedef unsigned u32x2 __attribute__((ext_vector_type(2)));
typedef __bf16 bfx2 __attribute__((ext_vector_type(2)));
typedef __attribute__((address_space(3))) s16x4* lds_s16x4_ptr;
typedef __attribute__((address_space(3))) unsigned* lds_u32_ptr;

#if defined(__HIP_DEVICE_COMPILE__)
#define NOPK __attribute__((target("no-packed-fp32-ops")))
#else
#define NOPK
#endif
#define DI NOPK __device__ __forceinline__

constexpr int S_ = 2048, M_ = 512, H_ = 32, D_ = 128, HD_ = 4096, FF_ = 1792;

DI float u2f(unsigned u) { return __builtin_bit_cast(float, u); }
DI unsigned f2u(float f) { return __builtin_bit_cast(unsigned, f); }
#define SYNC() asm volatile("s_waitcnt vmcnt(0) lgkmcnt(0)\n\ts_barrier" ::: "memory")
DI float shx(float v, int o) {
  const int lane = __builtin_amdgcn_mbcnt_hi(~0u, __builtin_amdgcn_mbcnt_lo(~0u, 0u));
  return u2f((unsigned)__builtin_amdgcn_ds_bpermute((lane ^ o) << 2, (int)f2u(v)));
}
DI bool any_lane(bool p) { return __builtin_amdgcn_ballot_w64(p) != 0ull; }
DI unsigned pk_bf16(float lo, float hi) {
  f32x2 v = {lo, hi};
  bfx2 r = __builtin_convertvector(v, bfx2);
  return __builtin_bit_cast(unsigned, r);
}
DI float bf_lo(unsigned u) { return u2f(u << 16); }
DI float bf_hi(unsigned u) { return u2f(u & 0xffff0000u); }
DI float bf2f(bf16_t v) { return u2f(((unsigned)v) << 16); }
DI float rbf(float x) { return bf_lo(pk_bf16(x, 0.f)); }
DI f32x16 mfma32(bf16x8 a, bf16x8 b, f32x16 c) { return __builtin_amdgcn_mfma_f32_32x32x16_bf16(a, b, c, 0, 0, 0); }
#define PIN(x) asm volatile("" : "+v"(x))
DI bf16x8 tr2(const char* p0, const char* p1) {
  s16x4 a = __builtin_amdgcn_ds_read_tr16_b64_v4i16((lds_s16x4_ptr)(p0));
  s16x4 b = __builtin_amdgcn_ds_read_tr16_b64_v4i16((lds_s16x4_ptr)(p1));
  return __builtin_shufflevector(a, b, 0, 1, 2, 3, 4, 5, 6, 7);
}
DI void glds16(const void* g, char* l) {
  __builtin_amdgcn_global_load_lds((const unsigned*)g, (lds_u32_ptr)l, 16, 0, 0);
}

NOPK __global__ __launch_bounds__(256) void mod_kernel(const bf16_t* __restrict__ c, const bf16_t* __restrict__ wmod, float* __restrict__ mod) {
  __shared__ float red[32][64];
  const int tid = threadIdx.x, cg = tid & 7, rg = tid >> 3, n0 = blockIdx.x * 64 + cg * 8;
  float acc[8] = {0, 0, 0, 0, 0, 0, 0, 0};
#pragma unroll
  for (int i = 0; i < 16; ++i) {
    const int k = rg + 32 * i;
    float cv = bf2f(c[k]);
    float s = rbf(cv / (1.f + __expf(-cv)));
    u32x4 w = *(const u32x4*)(wmod + (size_t)k * 3072 + n0);
#pragma unroll
    for (int j = 0; j < 4; ++j) { acc[2 * j] += s * bf_lo(w[j]); acc[2 * j + 1] += s * bf_hi(w[j]); }
  }
#pragma unroll
  for (int j = 0; j < 8; ++j) red[rg][cg * 8 + j] = acc[j];
  SYNC();
  if (tid < 64) {
    float s = 0;
#pragma unroll
    for (int i = 0; i < 32; ++i) s += red[i][tid];
    mod[blockIdx.x * 64 + tid] = rbf(s);
  }
}

NOPK __global__ __launch_bounds__(256) void ln_kernel(const bf16_t* __restrict__ x, const float* __restrict__ shift, const float* __restrict__ scale, bf16_t* __restrict__ h) {
  const int lane = threadIdx.x & 63, row = 256 * (blockIdx.x & 7) + 4 * (blockIdx.x >> 3) + (threadIdx.x >> 6);
  u32x4 v = *(const u32x4*)(x + (size_t)row * M_ + lane * 8);
  float f[8];
#pragma unroll
  for (int j = 0; j < 4; ++j) { f[2 * j] = bf_lo(v[j]); f[2 * j + 1] = bf_hi(v[j]); }
  float s = 0;
#pragma unroll
  for (int j = 0; j < 8; ++j) s += f[j];
#pragma unroll
  for (int o = 32; o; o >>= 1) s += shx(s, o);
  const float mean = s * (1.f / M_);
  float q = 0;
#pragma unroll
  for (int j = 0; j < 8; ++j) { f[j] -= mean; q += f[j] * f[j]; }
#pragma unroll
  for (int o = 32; o; o >>= 1) q += shx(q, o);
  const float rstd = rsqrtf(q * (1.f / M_) + 1e-5f);
  u32x4 o;
#pragma unroll
  for (int j = 0; j < 4; ++j) {
    const int n = lane * 8 + 2 * j;
    float a = f[2 * j] * rstd * rbf(1.f + scale[n]) + shift[n];
    float b = f[2 * j + 1] * rstd * rbf(1.f + scale[n + 1]) + shift[n + 1];
    o[j] = pk_bf16(a, b);
  }
  *(u32x4*)(h + (size_t)row * M_ + lane * 8) = o;
}

enum { EPI_PLAIN = 0, EPI_RELU = 1, EPI_RES = 2 };
#define WAIT_VM(n) asm volatile("s_waitcnt vmcnt(%0)" ::"i"(n) : "memory")
#define WAIT_LGKM0() asm volatile("s_waitcnt lgkmcnt(0)" ::: "memory")

struct NoSide { DI void pre() {} DI void post() {} };
template <int BM, int BN, int WM, int WN, int BK, int NS, int EPI, class Side = NoSide>
DI void gemm_body(const bf16_t* __restrict__ A, int lda, const bf16_t* __restrict__ B, int ldb, bf16_t* __restrict__ C, int ldc, int K,
                  int m0, int n0, char* smem, Side side = Side()) {
  constexpr int NW = WM * WN;
  constexpr int WTM = BM / WM, WTN = BN / WN, TM = WTM / 32, TN = WTN / 32;
  constexpr int AROW = BK * 2, BROW = BN * 2;
  constexpr int A_BYTES = BM * AROW, B_BYTES = BK * BROW, BUF = A_BYTES + B_BYTES;
  constexpr int A_INSTR = A_BYTES / 1024, B_INSTR = B_BYTES / 1024;
  static_assert(A_INSTR % NW == 0 && B_INSTR % NW == 0, "staging split");
  constexpr int PER = (A_INSTR + B_INSTR) / NW;
  constexpr int ACH = AROW / 16;
  const int tid = threadIdx.x, lane = tid & 63, w = __builtin_amdgcn_readfirstlane(tid >> 6);
  const int wm = w / WN, wn = w % WN, r32 = lane & 31, hi = lane >> 5;

  const bf16_t* asrc[A_INSTR / NW];
  const bf16_t* bsrc[B_INSTR / NW];
#pragma unroll
  for (int i = 0; i < A_INSTR / NW; ++i) {
    const int piece = w + i * NW, u = piece * 64 + lane, r = u / ACH, cc = u % ACH;
    const int ch = cc ^ (BK == 64 ? ((r >> 1) & 7) : ((r >> 2) & 3));
    asrc[i] = A + (size_t)(m0 + r) * lda + ch * 8;
  }
  constexpr int UPR = BN / 8;
#pragma unroll
  for (int i = 0; i < B_INSTR / NW; ++i) {
    const int piece = w + i * NW, u = piece * 64 + lane, r = u / UPR, uu = u % UPR;
    const int c = (uu >> 2) ^ (BN >= 128 ? (r & 3) : ((r >> 1) & 1));
    bsrc[i] = B + (size_t)r * ldb + n0 + (c * 4 + (uu & 3)) * 8;
  }
  auto stage = [&](int t, int buf) NOPK {
    char* base = smem + buf * BUF;
#pragma unroll
    for (int i = 0; i < A_INSTR / NW; ++i) glds16(asrc[i] + t * BK, base + (w + i * NW) * 1024);
#pragma unroll
    for (int i = 0; i < B_INSTR / NW; ++i) glds16(bsrc[i] + (size_t)t * BK * ldb, base + A_BYTES + (w + i * NW) * 1024);
  };

  f32x16 acc[TM][TN];
#pragma unroll
  for (int i = 0; i < TM; ++i)
#pragma unroll
    for (int j = 0; j < TN; ++j)
#pragma unroll
      for (int r = 0; r < 16; ++r) acc[i][j][r] = 0.f;
#pragma unroll
  for (int i = 0; i < TM; ++i)
#pragma unroll
    for (int j = 0; j < TN; ++j) PIN(acc[i][j]);

  const int asw = BK == 64 ? ((lane >> 1) & 7) : ((lane >> 2) & 3);
  const int a_off = (wm * WTM + r32) * AROW;
  const int q = (lane & 15) >> 2, p = lane & 3, chalf = (lane >> 4) & 1;
  const int bsw = BN >= 128 ? q : (q >> 1);
  const int b_row = (8 * hi + q) * BROW + (chalf * 16 + 4 * p) * 2;
  const int nt = K / BK;
#pragma unroll
  for (int t = 0; t < NS - 1; ++t) stage(t, t);
  side.pre();
#define PP_BAR() asm volatile("s_barrier" ::: "memory")
  static_assert(WM == 2, "ping-pong needs two wave groups");
  constexpr int KSN = BK / 16;
  bf16x8 fa[KSN][TM], fb[KSN][TN];
  WAIT_VM(PER * (NS - 2));
  PP_BAR();
  if (wm == 1) PP_BAR();
  int buf = 0;
  for (int t = 0; t < nt; ++t) {
    {
      const char* As = smem + buf * BUF;
      const char* Bs = As + A_BYTES;
#pragma unroll
      for (int s = 0; s < KSN; ++s) {
#pragma unroll
        for (int i = 0; i < TM; ++i) fa[s][i] = *(const bf16x8*)(As + a_off + i * 32 * AROW + (((2 * s + hi) ^ asw) << 4));
#pragma unroll
        for (int j = 0; j < TN; ++j) {
          const int cj = (wn * WTN) / 32 + j;
          const char* pb = Bs + b_row + ((cj ^ bsw) << 6) + (16 * s) * BROW;
          const unsigned pa_ = (unsigned)(uintptr_t)pb;
          s16x4 lo_, hi_;
          asm volatile("ds_read_b64_tr_b16 %0, %1" : "=v"(lo_) : "v"(pa_));
          asm volatile("ds_read_b64_tr_b16 %0, %1 offset:%2" : "=v"(hi_) : "v"(pa_), "i"(4 * BROW));
          fb[s][j] = __builtin_shufflevector(lo_, hi_, 0, 1, 2, 3, 4, 5, 6, 7);
        }
      }
      if (t + NS - 1 < nt) { int nb = buf + NS - 1; if (nb >= NS) nb -= NS; stage(t + NS - 1, nb); }
    }
    if (wm == 1) { if (t + NS - 1 < nt) WAIT_VM(PER * (NS - 2)); else WAIT_VM(0); }
    asm volatile("s_waitcnt lgkmcnt(0)\n\ts_barrier" ::: "memory");
#pragma unroll
    for (int s = 0; s < KSN; ++s) {
#pragma unroll
      for (int i = 0; i < TM; ++i) asm volatile("" : "+v"(fa[s][i]));
#pragma unroll
      for (int j = 0; j < TN; ++j) asm volatile("" : "+v"(fb[s][j]));
    }
    __builtin_amdgcn_sched_barrier(0);
    __builtin_amdgcn_s_setprio(1);
#pragma unroll
    for (int s = 0; s < KSN; ++s)
#pragma unroll
      for (int i = 0; i < TM; ++i)
#pragma unroll
        for (int j = 0; j < TN; ++j) acc[i][j] = mfma32(fb[s][j], fa[s][i], acc[i][j]);
    __builtin_amdgcn_s_setprio(0);
    __builtin_amdgcn_sched_barrier(0);
    if (wm == 0) { if (t + NS - 1 < nt) WAIT_VM(PER * (NS - 2)); else WAIT_VM(0); }
    PP_BAR();
    ++buf; if (buf == NS) buf = 0;
  }
  if (wm == 0) PP_BAR();
  SYNC();
  constexpr int ERS = WTN * 2 + 16;
  char* ep = smem + w * (WTM * ERS);
#pragma unroll
  for (int i = 0; i < TM; ++i)
#pragma unroll
    for (int j = 0; j < TN; ++j)
#pragma unroll
      for (int gq = 0; gq < 4; ++gq) {
        float v0 = acc[i][j][4 * gq], v1 = acc[i][j][4 * gq + 1], v2 = acc[i][j][4 * gq + 2], v3 = acc[i][j][4 * gq + 3];
        if (EPI == EPI_RELU) { v0 = fmaxf(v0, 0.f); v1 = fmaxf(v1, 0.f); v2 = fmaxf(v2, 0.f); v3 = fmaxf(v3, 0.f); }
        u32x2 o = {pk_bf16(v0, v1), pk_bf16(v2, v3)};
        *(u32x2*)(ep + (i * 32 + r32) * ERS + (j * 32 + 8 * gq + 4 * hi) * 2) = o;
      }
  constexpr int LPR = WTN / 8, RPI = 64 / LPR;
  const int er = lane / LPR, ec = lane % LPR;
#pragma unroll
  for (int it = 0; it < WTM / RPI; ++it) {
    const int r = it * RPI + er;
    u32x4 v = *(const u32x4*)(ep + r * ERS + ec * 16);
    if (EPI == EPI_RELU) {
      const int row = m0 + wm * WTM + r, ch = (n0 + wn * WTN) / 8 + ec;
      *(u32x4*)(C + ((size_t)((row >> 5) * ldc + (ch >> 1)) * 64 + (row & 31) + 32 * (ch & 1)) * 8) = v;
    } else {
      *(u32x4*)(C + (size_t)(m0 + wm * WTM + r) * ldc + n0 + wn * WTN + ec * 8) = v;
    }
  }
  side.post();
}

template <int BM, int BN, int WM, int WN, int BK, int NS, int EPI>
constexpr int gemm_smem() {
  constexpr int a = NS * (BM * BK * 2 + BK * BN * 2), b = WM * WN * (BM / WM) * ((BN / WN) * 2 + 16);
  return a > b ? a : b;
}

template <int BM, int BN, int WM, int WN, int BK, int NS, int EPI>
NOPK __global__ __launch_bounds__(WM* WN * 64) void gemm_kernel(const bf16_t* A, const bf16_t* B, bf16_t* C, int lda, int ldb, int ldc, int K, int ntn) {
  extern __shared__ __attribute__((aligned(16))) char smem[];
  const int idx = blockIdx.x >> 3, bm = 2 * (blockIdx.x & 7) + idx / ntn, bn = idx % ntn;
  gemm_body<BM, BN, WM, WN, BK, NS, EPI>(A, lda, B, ldb, C, ldc, K, bm * BM, bn * BN, smem);
}

struct KvSide {
  const bf16_t* c; const bf16_t* wmod; float* mod; bf16_t* modb; const bf16_t* x; float* stats; bf16_t* xt; char* scratch;
  const bf16_t* pfw[4];
  u32x4 xv, w0, w1; bf16_t cb; unsigned pf[4];
  DI void pre() {
    const int tid = threadIdx.x, lane = tid & 63, w = tid >> 6, b = blockIdx.x;
    xv = *(const u32x4*)(x + (size_t)(b * 8 + w) * M_ + lane * 8);
    const int bb = b < 192 ? b : 0;
    cb = c[tid];
    w0 = *(const u32x4*)(wmod + (size_t)tid * 3072 + bb * 16); w1 = *(const u32x4*)(wmod + (size_t)tid * 3072 + bb * 16 + 8);
    const int gl = b * 512 + tid;
    pf[0] = gl < 0 ? *(const unsigned*)(pfw[0] + (size_t)gl * 64) : 0u;
    pf[1] = (gl < 0) ? *(const unsigned*)(pfw[1] + (size_t)(gl - 32768) * 64) : 0u;
    pf[2] = (gl < 0) ? *(const unsigned*)(pfw[2] + (size_t)(gl - 65536) * 64) : 0u;
    pf[3] = (gl < 0) ? *(const unsigned*)(pfw[3] + (size_t)(gl - 81920) * 64) : 0u;
  }
  DI void post() {
    const int tid = threadIdx.x, lane = tid & 63, w = tid >> 6, b = blockIdx.x;
    asm volatile("" ::"v"(pf[0]), "v"(pf[1]), "v"(pf[2]), "v"(pf[3]));
    {
      const int row = b * 8 + w;
      *(u32x4*)(xt + ((size_t)((row >> 5) * 32 + (lane >> 1)) * 64 + (row & 31) + 32 * (lane & 1)) * 8) = xv;
      float f[8];
#pragma unroll
      for (int j = 0; j < 4; ++j) { f[2 * j] = bf_lo(xv[j]); f[2 * j + 1] = bf_hi(xv[j]); }
      float sm = 0;
#pragma unroll
      for (int j = 0; j < 8; ++j) sm += f[j];
#pragma unroll
      for (int o = 32; o; o >>= 1) sm += shx(sm, o);
      const float mean = sm * (1.f / M_);
      float q = 0;
#pragma unroll
      for (int j = 0; j < 8; ++j) { const float d = f[j] - mean; q += d * d; }
#pragma unroll
      for (int o = 32; o; o >>= 1) q += shx(q, o);
      if (lane == 0) { f32x2 st = {mean, rsqrtf(q * (1.f / M_) + 1e-5f)}; *(f32x2*)(stats + 2 * row) = st; }
    }
    if (b < 192) {
      const float cv = bf2f(cb);
      const float sv = rbf(cv / (1.f + __expf(-cv)));
      float a[16];
#pragma unroll
      for (int j = 0; j < 4; ++j) { a[2 * j] = sv * bf_lo(w0[j]); a[2 * j + 1] = sv * bf_hi(w0[j]); a[8 + 2 * j] = sv * bf_lo(w1[j]); a[8 + 2 * j + 1] = sv * bf_hi(w1[j]); }
      float b8[8], b4[4], b2[2], b1;
      {
        const bool up = lane & 32;
#pragma unroll
        for (int j = 0; j < 8; ++j) { const float keep = up ? a[8 + j] : a[j], give = up ? a[j] : a[8 + j]; b8[j] = keep + shx(give, 32); }
      }
      {
        const bool up = lane & 16;
#pragma unroll
        for (int j = 0; j < 4; ++j) { const float keep = up ? b8[4 + j] : b8[j], give = up ? b8[j] : b8[4 + j]; b4[j] = keep + shx(give, 16); }
      }
      {
        const bool up = lane & 8;
#pragma unroll
        for (int j = 0; j < 2; ++j) { const float keep = up ? b4[2 + j] : b4[j], give = up ? b4[j] : b4[2 + j]; b2[j] = keep + shx(give, 8); }
      }
      {
        const bool up = lane & 4;
        const float keep = up ? b2[1] : b2[0], give = up ? b2[0] : b2[1];
        b1 = keep + shx(give, 4);
      }
      b1 += shx(b1, 2);
      b1 += shx(b1, 1);
      SYNC();
      float* red = (float*)scratch;
      if ((lane & 3) == 0) red[w * 16 + (lane >> 2)] = b1;
      SYNC();
      if (tid < 16) {
        float t = 0;
#pragma unroll
        for (int i = 0; i < 8; ++i) t += red[i * 16 + tid];
        const int n = b * 16 + tid;
        const float r = rbf(t);
        mod[n] = r;
        float* mf = (float*)modb;
        if (n < 512) mf[n] = r;
        else if (n < 1024) mf[n] = rbf(1.f + r);
        else if (n >= 1536 && n < 2048) mf[n - 512] = r;
        else if (n >= 2048 && n < 2560) mf[n - 512] = rbf(1.f + r);
      }
    }
  }
};
template <int BM, int BN, int WM, int WN, int BK, int NS>
NOPK __global__ __launch_bounds__(WM* WN * 64) void kv_kernel(const bf16_t* ckv, const bf16_t* wk, const bf16_t* wv, bf16_t* k, bf16_t* v,
                                                          const bf16_t* c, const bf16_t* wmod, float* mod, bf16_t* modb, const bf16_t* x, float* stats, bf16_t* xt,
                                                          const bf16_t* p0, const bf16_t* p1, const bf16_t* p2, const bf16_t* p3) {
  extern __shared__ __attribute__((aligned(16))) char smem[];
  constexpr int NTN = HD_ / BN, NTM = S_ / BM, PER = NTN * NTM;
  static_assert(2 * PER == 256 && WM * WN == 8, "side jobs assume 256 blocks of 512 threads");
  const int xcd = blockIdx.x & 7, idx = blockIdx.x >> 3;
  const int which = idx >> 4, b = (2 * xcd + ((idx >> 3) & 1)) * NTM + (idx & 7);
  const bf16_t* B = which == 0 ? wk : wv;
  bf16_t* C = which == 0 ? k : v;
  KvSide side; side.c = c; side.wmod = wmod; side.mod = mod; side.modb = modb; side.x = x; side.stats = stats; side.xt = xt; side.scratch = smem;
  side.pfw[0] = p0; side.pfw[1] = p1; side.pfw[2] = p2; side.pfw[3] = p3;
  gemm_body<BM, BN, WM, WN, BK, NS, EPI_PLAIN, KvSide>(ckv, M_, B, HD_, C, HD_, M_, (b % NTM) * BM, (b / NTM) * BN, smem, side);
}

template <int NKS>
NOPK __global__ __launch_bounds__(512) void gemm_rs(const bf16_t* __restrict__ At, const bf16_t* __restrict__ Bt, bf16_t* __restrict__ C,
                                                    const bf16_t* __restrict__ res, const float* __restrict__ gate) {
  extern __shared__ __attribute__((aligned(16))) char smem[];
  constexpr int KS = NKS * 8;
  constexpr int D = NKS < 8 ? NKS : 8;
  const int tid = threadIdx.x, lane = tid & 63, w = __builtin_amdgcn_readfirstlane(tid >> 6);
  const int r32 = lane & 31, hi = lane >> 5;
  const int xcd = blockIdx.x & 7, idx = blockIdx.x >> 3;
  const int mb = xcd * 4 + (idx >> 3), nb = idx & 7;
  const auto ra = __builtin_amdgcn_make_buffer_rsrc((void*)At, 0, S_ * KS * 32, 0x00020000);
  const auto rb = __builtin_amdgcn_make_buffer_rsrc((void*)Bt, 0, KS * 16 * M_ * 2, 0x00020000);
  const int vo = lane * 16;
  const int sa0 = ((2 * mb) * KS + w * NKS) * 1024, sa1 = sa0 + KS * 1024;
  const int sb0 = ((w * NKS) * 16 + 2 * nb) * 1024;
  f32x16 acc[2][2];
#pragma unroll
  for (int i = 0; i < 2; ++i)
#pragma unroll
    for (int j = 0; j < 2; ++j)
#pragma unroll
      for (int r = 0; r < 16; ++r) acc[i][j][r] = 0.f;
  PIN(acc[0][0]); PIN(acc[0][1]); PIN(acc[1][0]); PIN(acc[1][1]);
  u32x4 fa[D][2], fb[D][2];
#define RS_LOAD(SL, T) do { \
    fa[SL][0] = __builtin_amdgcn_raw_buffer_load_b128(ra, vo, sa0 + (T) * 1024, 0); \
    fb[SL][0] = __builtin_amdgcn_raw_buffer_load_b128(rb, vo, sb0 + (T) * 16384, 0); \
    fa[SL][1] = __builtin_amdgcn_raw_buffer_load_b128(ra, vo, sa1 + (T) * 1024, 0); \
    fb[SL][1] = __builtin_amdgcn_raw_buffer_load_b128(rb, vo, sb0 + (T) * 16384 + 1024, 0); } while (0)
#pragma unroll
  for (int t = 0; t < D; ++t) RS_LOAD(t, t);
  __builtin_amdgcn_sched_barrier(0);
#pragma unroll
  for (int t = 0; t < NKS; ++t) {
    const int sl = t % D;
#pragma unroll
    for (int i = 0; i < 2; ++i)
#pragma unroll
      for (int j = 0; j < 2; ++j)
        acc[i][j] = mfma32(__builtin_bit_cast(bf16x8, fb[sl][j]), __builtin_bit_cast(bf16x8, fa[sl][i]), acc[i][j]);
    __builtin_amdgcn_sched_barrier(0);
    if (t + D < NKS) RS_LOAD(sl, t + D);
    __builtin_amdgcn_sched_barrier(0);
  }
  constexpr int RS = 272;
  {
    char* pw = smem + w * (64 * RS);
#pragma unroll
    for (int i = 0; i < 2; ++i)
#pragma unroll
      for (int j = 0; j < 2; ++j)
#pragma unroll
        for (int gq = 0; gq < 4; ++gq) {
          f32x4 v = {acc[i][j][4 * gq], acc[i][j][4 * gq + 1], acc[i][j][4 * gq + 2], acc[i][j][4 * gq + 3]};
          *(f32x4*)(pw + (i * 32 + r32) * RS + (j * 32 + 8 * gq + 4 * hi) * 4) = v;
        }
  }
  SYNC();
  {
    const int m0 = mb * 64, n0 = nb * 64;
    const int m = tid >> 3, nn = (tid & 7) * 8;
    f32x4 s0 = {0, 0, 0, 0}, s1 = {0, 0, 0, 0};
#pragma unroll
    for (int pp = 0; pp < 8; ++pp) {
      const char* src = smem + pp * (64 * RS) + m * RS + nn * 4;
      s0 += *(const f32x4*)src; s1 += *(const f32x4*)(src + 16);
    }
    const u32x4 rr = *(const u32x4*)(res + (size_t)(m0 + m) * M_ + n0 + nn);
    const f32x4 g0 = *(const f32x4*)(gate + n0 + nn), g1 = *(const f32x4*)(gate + n0 + nn + 4);
    u32x4 o;
    o[0] = pk_bf16(bf_lo(rr[0]) + rbf(g0[0] * rbf(s0[0])), bf_hi(rr[0]) + rbf(g0[1] * rbf(s0[1])));
    o[1] = pk_bf16(bf_lo(rr[1]) + rbf(g0[2] * rbf(s0[2])), bf_hi(rr[1]) + rbf(g0[3] * rbf(s0[3])));
    o[2] = pk_bf16(bf_lo(rr[2]) + rbf(g1[0] * rbf(s1[0])), bf_hi(rr[2]) + rbf(g1[1] * rbf(s1[1])));
    o[3] = pk_bf16(bf_lo(rr[3]) + rbf(g1[2] * rbf(s1[2])), bf_hi(rr[3]) + rbf(g1[3] * rbf(s1[3])));
    *(u32x4*)(C + (size_t)(m0 + m) * M_ + n0 + nn) = o;
  }
}

constexpr float ATT_C2 = 0.08838834764831845f * 1.4426950408889634f;
#define DSR128(dst, addr, off) asm volatile("ds_read_b128 %0, %1 offset:%2" : "=v"(dst) : "v"(addr), "i"(off))
#define DSRTR(dst, addr, off) asm volatile("ds_read_b64_tr_b16 %0, %1 offset:%2" : "=v"(dst) : "v"(addr), "i"(off))
#define SB0() __builtin_amdgcn_sched_barrier(0)
DI void glds_s(const char* sbase, unsigned voff, unsigned lds_addr) {
  asm volatile("s_mov_b32 m0, %2\n\ts_nop 0\n\tglobal_load_lds_dwordx4 %0, %1" ::"v"(voff), "s"(sbase), "s"(lds_addr) : "memory");
}
#define K_READ2(F, S0) do { const unsigned a0_ = kaddr ^ ((S0) << 5), a1_ = kaddr ^ (((S0) + 1) << 5); \
    DSR128(F[0], a0_, 0); DSR128(F[1], a0_, 8192); DSR128(F[2], a1_, 0); DSR128(F[3], a1_, 8192); } while (0)
#define WAIT4(N, F) asm volatile("s_waitcnt lgkmcnt(" #N ")" : "+v"(F[0]), "+v"(F[1]), "+v"(F[2]), "+v"(F[3]))
#define K_MMA2(F, S0) do { \
    _Pragma("unroll") for (int s_ = 0; s_ < 2; ++s_) { s0 = mfma32(F[2 * s_], qr[S0 + s_], s0); s1 = mfma32(F[2 * s_ + 1], qr[S0 + s_], s1); } } while (0)
#define ATT_QK(HOOK) do { \
  bf16x8 fa[4], fb[4]; \
  K_READ2(fa, 0); \
  K_READ2(fb, 2); \
  if (last) { int dq_ = dq_last; asm volatile("" : "+v"(dq_)); \
    _Pragma("unroll") for (int r = 0; r < 16; ++r) { const int c_ = (r & 3) + 8 * (r >> 2); \
      s0[r] = c_ > dq_ ? -__builtin_inff() : 0.f; s1[r] = c_ + 32 > dq_ ? -__builtin_inff() : 0.f; } \
  } else { _Pragma("unroll") for (int r = 0; r < 16; ++r) { s0[r] = 0.f; s1[r] = 0.f; } } \
  WAIT4(4, fa); SB0(); \
  K_MMA2(fa, 0); SB0(); \
  K_READ2(fa, 4); HOOK(0); \
  WAIT4(4, fb); SB0(); \
  K_MMA2(fb, 2); SB0(); \
  K_READ2(fb, 6); HOOK(1); \
  WAIT4(4, fa); SB0(); \
  K_MMA2(fa, 4); SB0(); HOOK(2); \
  WAIT4(0, fb); SB0(); \
  K_MMA2(fb, 6); SB0(); HOOK(3); } while (0)
struct VFrag { s16x4 lo[4], hi[4]; };
#define V_READ1(F, S) do { \
      DSRTR(F.lo[0], va0, (S) * 4096); DSRTR(F.hi[0], va0, (S) * 4096 + 2048); \
      DSRTR(F.lo[1], va1, (S) * 4096); DSRTR(F.hi[1], va1, (S) * 4096 + 2048); \
      DSRTR(F.lo[2], va2, (S) * 4096); DSRTR(F.hi[2], va2, (S) * 4096 + 2048); \
      DSRTR(F.lo[3], va3, (S) * 4096); DSRTR(F.hi[3], va3, (S) * 4096 + 2048); } while (0)
#define VWAIT(N, F) asm volatile("s_waitcnt lgkmcnt(" #N ")" : "+v"(F.lo[0]), "+v"(F.lo[1]), "+v"(F.lo[2]), "+v"(F.lo[3]), "+v"(F.hi[0]), "+v"(F.hi[1]), "+v"(F.hi[2]), "+v"(F.hi[3]))
#define V_MMA1(F, S) do { const bf16x8 pb_ = __builtin_bit_cast(bf16x8, pw[S]); \
      _Pragma("unroll") for (int d_ = 0; d_ < 4; ++d_) \
        o[d_] = mfma32(__builtin_shufflevector(F.lo[d_], F.hi[d_], 0, 1, 2, 3, 4, 5, 6, 7), pb_, o[d_]); } while (0)
#define V_ADDR() const unsigned va0 = vaddr, va1 = vaddr ^ 64, va2 = vaddr ^ 128, va3 = vaddr ^ 192
#define PV_TAIL(HOOK) do { \
      VWAIT(8, f0); SB0(); V_MMA1(f0, 0); SB0(); V_READ1(f0, 2); HOOK(0); \
      VWAIT(8, f1); SB0(); V_MMA1(f1, 1); SB0(); V_READ1(f1, 3); HOOK(1); \
      VWAIT(8, f0); SB0(); V_MMA1(f0, 2); SB0(); HOOK(2); \
      VWAIT(0, f1); SB0(); V_MMA1(f1, 3); SB0(); HOOK(3); } while (0)
#define ATT_SM() do { \
  float mx = s0[0]; \
  _Pragma("unroll") for (int r = 1; r < 16; ++r) mx = fmaxf(mx, s0[r]); \
  _Pragma("unroll") for (int r = 0; r < 16; ++r) mx = fmaxf(mx, s1[r]); \
  { auto rr = __builtin_amdgcn_permlane32_swap(f2u(mx), f2u(mx), false, false); \
    mx = fmaxf(u2f(rr[0]), u2f(rr[1])); } \
    \
  if (any_lane((mx - m) * ATT_C2 > 8.f)) { \
    const float mnew = fmaxf(m, mx); \
    const float alpha = __builtin_amdgcn_exp2f((m - mnew) * ATT_C2); \
    m = mnew; l *= alpha; \
    _Pragma("unroll") for (int d = 0; d < 4; ++d) _Pragma("unroll") for (int r = 0; r < 16; ++r) o[d][r] *= alpha; } \
  const float mc = -m * ATT_C2; \
  float ps = 0.f; \
  _Pragma("unroll") for (int r = 0; r < 16; ++r) { s0[r] = __builtin_amdgcn_exp2f(fmaf(s0[r], ATT_C2, mc)); ps += s0[r]; } \
  _Pragma("unroll") for (int r = 0; r < 16; ++r) { s1[r] = __builtin_amdgcn_exp2f(fmaf(s1[r], ATT_C2, mc)); ps += s1[r]; } \
  l += ps; \
  _Pragma("unroll") for (int s = 0; s < 4; ++s) _Pragma("unroll") for (int j = 0; j < 4; ++j) { \
      const int r = 8 * (s & 1) + 2 * j; \
      pw[s][j] = s < 2 ? pk_bf16(s0[r], s0[r + 1]) : pk_bf16(s1[r], s1[r + 1]); } } while (0)
#define ATT_BAR() asm volatile("s_waitcnt vmcnt(0) lgkmcnt(0)\n\ts_barrier" ::: "memory")
#define NOHOOK(i) do {} while (0)

NOPK __global__ __launch_bounds__(512) void attn_kernel(const bf16_t* __restrict__ X, const float* __restrict__ stats, const bf16_t* __restrict__ modb,
                                                   const bf16_t* __restrict__ Wq, const bf16_t* __restrict__ Kg, const bf16_t* __restrict__ Vg, bf16_t* __restrict__ ctx,
                                                   const bf16_t* __restrict__ Wo, const bf16_t* __restrict__ W2, bf16_t* __restrict__ Wot, bf16_t* __restrict__ W2t) {
  extern __shared__ __attribute__((aligned(16))) char smem[];
  const int tid = threadIdx.x, lane = tid & 63, w = __builtin_amdgcn_readfirstlane(tid >> 6), g = w >> 2, wq = w & 3;
  const int r32 = lane & 31, hi = lane >> 5;
  const int b = blockIdx.x, head = 4 * (b & 7) + (b >> 6), pair = (b >> 3) & 7;
  const unsigned lds0 = (unsigned)(uintptr_t)smem;
  const unsigned Kl = lds0 + g * 65536, Vl = Kl + 32768;
  const int srow = 4 * wq + (lane >> 4), su = lane & 15;
  const unsigned ksrc = (unsigned)(srow * HD_ + head * D_ + ((su ^ srow) << 3)) * 2u;
  const unsigned vsrc = (unsigned)(srow * HD_ + head * D_ + (((((su >> 2) ^ (srow & 3)) << 2) | (su & 3)) << 3)) * 2u;
#define STAGE_K1(tile, buf, i) glds_s((const char*)Kg + (size_t)((tile) * 64 + 16 * (i)) * (HD_ * 2), ksrc, Kl + (buf) * 16384 + wq * 1024 + (i) * 4096)
#define STAGE_V1(tile, buf, i) glds_s((const char*)Vg + (size_t)((tile) * 64 + 16 * (i)) * (HD_ * 2), vsrc, Vl + (buf) * 16384 + wq * 1024 + (i) * 4096)
  const int q4 = (lane & 15) >> 2, p4 = lane & 3, chalf = (lane >> 4) & 1;
  const unsigned kaddr0 = Kl + r32 * 256 + ((hi ^ (r32 & 15)) << 4);
  const unsigned vaddr0 = Vl + (4 * hi + q4) * 256 + (chalf * 16 + 4 * p4) * 2 + (q4 << 6);

  bf16x8 qr[8];
  char* Qs = smem + 131072;
  {
    {
      const int kr = lane >> 4;
      const unsigned wsrc = (unsigned)(kr * HD_ + head * D_ + (((((su >> 2) ^ kr) << 2) | (su & 3)) << 3)) * 2u;
#pragma unroll
      for (int i = 0; i < 16; ++i) glds_s((const char*)Wq + (size_t)((w * 16 + i) * 4) * (HD_ * 2), wsrc, lds0 + (w * 16 + i) * 1024);
    }
    const int boff = (8 * hi + q4) * 256 + (chalf * 16 + 4 * p4) * 2;
    const int xq = g == 0 ? 15 - pair : pair;
    const f32x2 stq = *(const f32x2*)(stats + 2 * (128 * xq + 32 * wq + r32));
    const float rs = stq[1], nm = -stq[0] * stq[1];
    const auto rx = __builtin_amdgcn_make_buffer_rsrc((void*)X, 0, S_ * M_ * 2, 0x00020000);
    const int voX = lane * 16;
    const int soX = (4 * xq + wq) * 32768;
    if (w < 4) glds_s((const char*)modb + w * 1024, (unsigned)voX, lds0 + 131072 + w * 1024);
    f32x16 qa[4];
#pragma unroll
    for (int r = 0; r < 16; ++r) { qa[0][r] = 0.f; qa[1][r] = 0.f; qa[2][r] = 0.f; qa[3][r] = 0.f; }
    u32x4 xa[8];
#pragma unroll
    for (int t = 0; t < 8; ++t) xa[t] = __builtin_amdgcn_raw_buffer_load_b128(rx, voX, soX + t * 1024, 0);
    ATT_BAR();
    unsigned short re[3][8];
#pragma unroll
    for (int i = 0; i < 3; ++i) {
      const int f0 = (blockIdx.x * 8 + w) + i * 2048, f = f0 < 4096 + 1792 ? f0 : 0;
      const bool first = f < 4096;
      const int ff = first ? f : f - 4096;
      const bf16_t* src = (first ? Wo : W2) + (size_t)((ff >> 4) * 16 + 8 * hi) * M_ + (ff & 15) * 32 + r32;
#pragma unroll
      for (int j = 0; j < 8; ++j) re[i][j] = src[(size_t)j * M_];
    }
    const unsigned wb_lo = lds0 + boff + (q4 << 6), wb_hi = wb_lo + 65536;
    s16x4 wl[2][4], wh[2][4];
#define W_READ(B_, KS_) do { const unsigned a_ = (KS_) < 16 ? wb_lo : wb_hi; \
      { DSRTR(wl[B_][0], a_, ((KS_) & 15) * 4096); DSRTR(wh[B_][0], a_, ((KS_) & 15) * 4096 + 1024); } \
      { const unsigned aj_ = a_ ^ 64; DSRTR(wl[B_][1], aj_, ((KS_) & 15) * 4096); DSRTR(wh[B_][1], aj_, ((KS_) & 15) * 4096 + 1024); } \
      { const unsigned aj_ = a_ ^ 128; DSRTR(wl[B_][2], aj_, ((KS_) & 15) * 4096); DSRTR(wh[B_][2], aj_, ((KS_) & 15) * 4096 + 1024); } \
      { const unsigned aj_ = a_ ^ 192; DSRTR(wl[B_][3], aj_, ((KS_) & 15) * 4096); DSRTR(wh[B_][3], aj_, ((KS_) & 15) * 4096 + 1024); } } while (0)
    const unsigned cfa = lds0 + 131072 + hi * 32;
    u32x4 cf[2][4];
#define C_READ(B_, KS_) do { DSR128(cf[B_][0], cfa, (KS_) * 64); DSR128(cf[B_][1], cfa, (KS_) * 64 + 16); \
      DSR128(cf[B_][2], cfa, 2048 + (KS_) * 64); DSR128(cf[B_][3], cfa, 2048 + (KS_) * 64 + 16); } while (0)
#define W_WAIT(N, B_) asm volatile("s_waitcnt lgkmcnt(" #N ")" : "+v"(wl[B_][0]), "+v"(wl[B_][1]), "+v"(wl[B_][2]), "+v"(wl[B_][3]), "+v"(wh[B_][0]), "+v"(wh[B_][1]), "+v"(wh[B_][2]), "+v"(wh[B_][3]), \
      "+v"(cf[B_][0]), "+v"(cf[B_][1]), "+v"(cf[B_][2]), "+v"(cf[B_][3]))
#define QP_STEP(KS_) do { \
        if ((KS_) + 1 < 32) { C_READ(((KS_) & 1) ^ 1, ((KS_) + 1) & 31); W_READ(((KS_) & 1) ^ 1, ((KS_) + 1) & 31); } \
        if ((KS_) + 1 < 32) W_WAIT(12, (KS_) & 1); else W_WAIT(0, (KS_) & 1); \
        u32x4 ha; \
        _Pragma("unroll") for (int j = 0; j < 4; ++j) \
          ha[j] = pk_bf16(fmaf(fmaf(bf_lo(xa[(KS_) & 7][j]), rs, nm), u2f(cf[(KS_) & 1][2 + (j >> 1)][(2 * j) & 3]), u2f(cf[(KS_) & 1][j >> 1][(2 * j) & 3])), \
                          fmaf(fmaf(bf_hi(xa[(KS_) & 7][j]), rs, nm), u2f(cf[(KS_) & 1][2 + (j >> 1)][(2 * j + 1) & 3]), u2f(cf[(KS_) & 1][j >> 1][(2 * j + 1) & 3]))); \
        const bf16x8 fa_ = __builtin_bit_cast(bf16x8, ha); \
        SB0(); \
        if ((KS_) + 8 < 32) xa[(KS_) & 7] = __builtin_amdgcn_raw_buffer_load_b128(rx, voX, soX + ((KS_) + 8) * 1024, 0); \
        _Pragma("unroll") for (int j = 0; j < 4; ++j) qa[j] = mfma32(__builtin_shufflevector(wl[(KS_) & 1][j], wh[(KS_) & 1][j], 0, 1, 2, 3, 4, 5, 6, 7), fa_, qa[j]); \
        SB0(); } while (0)
#define QP_4(K0) do { QP_STEP(K0); QP_STEP(K0 + 1); QP_STEP(K0 + 2); QP_STEP(K0 + 3); } while (0)
    C_READ(0, 0); W_READ(0, 0);
    QP_4(0); QP_4(4); QP_4(8); QP_4(12); QP_4(16); QP_4(20); QP_4(24); QP_4(28);
#pragma unroll
    for (int i = 0; i < 3; ++i) {
      const int f = (blockIdx.x * 8 + w) + i * 2048;
      if (f < 4096 + 1792) {
        const bool first = f < 4096;
        const int ff = first ? f : f - 4096;
        u32x4 pk;
#pragma unroll
        for (int j = 0; j < 4; ++j) pk[j] = (unsigned)re[i][2 * j] | ((unsigned)re[i][2 * j + 1] << 16);
        *(u32x4*)((first ? Wot : W2t) + ((size_t)ff * 64 + lane) * 8) = pk;
      }
    }
    asm volatile("s_waitcnt lgkmcnt(0)\n\ts_barrier" ::: "memory");
    const int lr = 32 * wq + r32;
    {
      char* dst = g == 0 ? smem : Qs;
#pragma unroll
      for (int j = 0; j < 4; ++j)
#pragma unroll
        for (int gq = 0; gq < 4; ++gq) {
          const int d = 32 * j + 8 * gq + 4 * hi;
          u32x2 ov = {pk_bf16(qa[j][4 * gq], qa[j][4 * gq + 1]), pk_bf16(qa[j][4 * gq + 2], qa[j][4 * gq + 3])};
          *(u32x2*)(dst + lr * 256 + ((((d >> 3) ^ (lr & 15))) << 4) + (d & 7) * 2) = ov;
        }
    }
    SYNC();
#pragma unroll
    for (int s = 0; s < 8; ++s) qr[s] = *(const bf16x8*)(smem + lr * 256 + (((2 * s + hi) ^ (lr & 15)) << 4));
    SYNC();
  }

  unsigned kaddr = kaddr0, vaddr = vaddr0;
  int p0 = 0;
#pragma unroll
  for (int i = 0; i < 4; ++i) STAGE_K1(g, 0, i);
  if (g == 0) {
#pragma unroll
    for (int i = 0; i < 4; ++i) STAGE_V1(0, 0, i);
  }
#pragma nounroll
  for (int pass = 0; pass < 2; ++pass) {
    const int x = pass == 0 ? 15 - pair : pair;
    const int nt = x + 1;
    const int qrow = 128 * x + 32 * wq + r32;
    f32x16 o[4], s0, s1;
    u32x4 pw[4];
    float m = -1e30f, l = 0.f;
    if (pass == 1) {
      const int lr = 32 * wq + r32;
#pragma unroll
      for (int s = 0; s < 8; ++s) qr[s] = *(const bf16x8*)(Qs + lr * 256 + (((2 * s + hi) ^ (lr & 15)) << 4));
    }
#pragma unroll
    for (int d = 0; d < 4; ++d)
#pragma unroll
      for (int r = 0; r < 16; ++r) o[d][r] = 0.f;
    if (pass == 0) ATT_BAR();
    const int dq_last = qrow - 64 * (2 * (nt - 1) + g) - 4 * hi;
    if (g == 0) {
#pragma nounroll
      for (int it = 0; it < nt; ++it) {
        const int buf = (it + p0) & 1;
        const bool last = it + 1 == nt;
        const int nx = last ? 0 : 2 * (it + 1);
#define HOOK_A(i) do { STAGE_K1(nx, buf ^ 1, i); STAGE_V1(nx, buf ^ 1, i); } while (0)
        ATT_QK(HOOK_A);
        VFrag f0, f1;
        V_ADDR();
        V_READ1(f0, 0); V_READ1(f1, 1);
#define BAL_MODE 0
#pragma unroll
        for (int i = 0; i < 4; ++i) {
          if (BAL_MODE <= 1) glds_s((const char*)Kg + (size_t)((nx + 1) * 64 + 16 * i) * (HD_ * 2), ksrc, Kl + 65536 + (buf ^ 1) * 16384 + wq * 1024 + i * 4096);
          if (BAL_MODE == 0) glds_s((const char*)Vg + (size_t)((2 * it + 1) * 64 + 16 * i) * (HD_ * 2), vsrc, Vl + 65536 + buf * 16384 + wq * 1024 + i * 4096);
        }
        SB0();
        ATT_SM();
        SB0();
        PV_TAIL(NOHOOK);
        kaddr ^= 16384; vaddr ^= 16384;
        ATT_BAR();
      }
    } else {
      const bool skip_last = wq < 2;
#pragma nounroll
      for (int it = 0; it < nt; ++it) {
        const int buf = (it + p0) & 1;
        const bool last = it + 1 == nt;
        if (it > 0) {
          VFrag f0, f1;
          V_ADDR();
          V_READ1(f0, 0); V_READ1(f1, 1);
          PV_TAIL(NOHOOK);
          vaddr ^= 16384;
        }
        const int nxb = last ? 1 : 2 * (it + 1) + 1;
#define HOOK_BV(i) do { if (BAL_MODE >= 1) STAGE_V1(2 * it + 1, buf, i); if (BAL_MODE == 2) STAGE_K1(nxb, buf ^ 1, i); } while (0)
        if (!(last && skip_last)) {
          ATT_QK(HOOK_BV);
          ATT_SM();
        } else {
          HOOK_BV(0); HOOK_BV(1); HOOK_BV(2); HOOK_BV(3);
        }
        kaddr ^= 16384;
        ATT_BAR();
      }
      if (!skip_last) {
        VFrag f0, f1;
        V_ADDR();
        V_READ1(f0, 0); V_READ1(f1, 1);
        PV_TAIL(NOHOOK);
      }
      vaddr ^= 16384;
    }
    ATT_BAR();
    const int pl = (nt - 1 + p0) & 1;
    l += shx(l, 32);
    float* xo = (float*)(smem + g * 65536 + (wq >> 1) * 32768 + pl * 16384 + (wq & 1) * 8192);
    float* sp = (float*)(smem + 65536 + 32768 + (pl ^ 1) * 16384 + w * 512);
#pragma unroll
    for (int dd = 0; dd < 2; ++dd)
#pragma unroll
      for (int r = 0; r < 16; ++r) xo[(dd * 16 + r) * 64 + lane] = g == 0 ? o[2 + dd][r] : o[dd][r];
    sp[lane] = m; sp[64 + lane] = l;
    SYNC();
    {
      const float* po = (const float*)(smem + (g ^ 1) * 65536 + (wq >> 1) * 32768 + pl * 16384 + (wq & 1) * 8192);
      const float* pst = (const float*)(smem + 65536 + 32768 + (pl ^ 1) * 16384 + (w ^ 4) * 512);
      const float mo = pst[lane], lo = pst[64 + lane];
      const float mt = fmaxf(m, mo);
      const float a1 = __builtin_amdgcn_exp2f((m - mt) * ATT_C2), a2 = __builtin_amdgcn_exp2f((mo - mt) * ATT_C2);
      const float inv = 1.f / (l * a1 + lo * a2);
      const float f1 = a1 * inv, f2 = a2 * inv;
      bf16_t* obase = ctx + ((size_t)((qrow >> 5) * 256 + head * 8 + g * 4) * 64 + r32) * 8 + 4 * hi;
#pragma unroll
      for (int dd = 0; dd < 2; ++dd)
#pragma unroll
        for (int gq = 0; gq < 4; ++gq) {
          float v[4];
#pragma unroll
          for (int e = 0; e < 4; ++e) {
            const int r = 4 * gq + e;
            const float own = g == 0 ? o[dd][r] : o[2 + dd][r];
            v[e] = own * f1 + po[(dd * 16 + r) * 64 + lane] * f2;
          }
          u32x2 ov = {pk_bf16(v[0], v[1]), pk_bf16(v[2], v[3])};
          *(u32x2*)(obase + ((dd * 2 + (gq >> 1)) * 64 + 32 * (gq & 1)) * 8) = ov;
        }
    }
    asm volatile("s_waitcnt lgkmcnt(0)\n\ts_barrier" ::: "memory");
    p0 = (p0 + nt) & 1;
  }
}

extern "C" void kernel_launch(void* const* d_in, const int* in_sizes, int n_in,
                              void* d_out, int out_size, void* d_ws, size_t ws_size,
                              hipStream_t stream) {
  const bf16_t* x = (const bf16_t*)d_in[0];
  const bf16_t* ckv = (const bf16_t*)d_in[1];
  const bf16_t* wq = (const bf16_t*)d_in[2];
  const bf16_t* wuk = (const bf16_t*)d_in[3];
  const bf16_t* wuv = (const bf16_t*)d_in[4];
  const bf16_t* wo = (const bf16_t*)d_in[5];
  const bf16_t* w1 = (const bf16_t*)d_in[7];
  const bf16_t* w2 = (const bf16_t*)d_in[8];
  const bf16_t* c = (const bf16_t*)d_in[9];
  const bf16_t* wmod = (const bf16_t*)d_in[10];
  bf16_t* out = (bf16_t*)d_out;

  char* ws = (char*)d_ws;
  float* mod = (float*)ws;
  bf16_t* h1 = (bf16_t*)(ws + (1 << 20));
  bf16_t* x1 = (bf16_t*)(ws + (4 << 20));
  bf16_t* h2 = (bf16_t*)(ws + (7 << 20));
  bf16_t* hid = (bf16_t*)(ws + (10 << 20));
  bf16_t* q = (bf16_t*)(ws + (32 << 20));
  bf16_t* k = (bf16_t*)(ws + (48 << 20));
  bf16_t* v = (bf16_t*)(ws + (64 << 20));
  bf16_t* ctx = (bf16_t*)(ws + (80 << 20));
  bf16_t* wot = (bf16_t*)(ws + (96 << 20));
  bf16_t* w2t = (bf16_t*)(ws + (100 << 20));

  bf16_t* modb = (bf16_t*)(ws + (32 << 10));
  float* stats = (float*)(ws + (64 << 10));
  {
    constexpr int BM = 256, BN = 256, WM = 2, WN = 4, BK = 32, NS = 4, SH = gemm_smem<BM, BN, WM, WN, BK, NS, 0>();
    (void)hipFuncSetAttribute((const void*)kv_kernel<BM, BN, WM, WN, BK, NS>, hipFuncAttributeMaxDynamicSharedMemorySize, SH);
    kv_kernel<BM, BN, WM, WN, BK, NS><<<256, WM * WN * 64, SH, stream>>>(ckv, wuk, wuv, k, v, c, wmod, mod, modb, x, stats, h1, wq, wo, w1, w2);
  }
  (void)hipFuncSetAttribute((const void*)attn_kernel, hipFuncAttributeMaxDynamicSharedMemorySize, 163840);
  attn_kernel<<<256, 512, 163840, stream>>>(h1, stats, modb, wq, k, v, ctx, wo, w2, wot, w2t);
  constexpr int RS_SH = 8 * 64 * 272;
  (void)hipFuncSetAttribute((const void*)gemm_rs<32>, hipFuncAttributeMaxDynamicSharedMemorySize, RS_SH);
  (void)hipFuncSetAttribute((const void*)gemm_rs<14>, hipFuncAttributeMaxDynamicSharedMemorySize, RS_SH);
  gemm_rs<32><<<256, 512, RS_SH, stream>>>(ctx, wot, x1, x, mod + 2 * 512);
  ln_kernel<<<S_ / 4, 256, 0, stream>>>(x1, mod + 3 * 512, mod + 4 * 512, h2);
  {
    constexpr int BM = 128, BN = 128, WM = 2, WN = 4, BK = 64, NS = 4, SH = gemm_smem<BM, BN, WM, WN, BK, NS, 0>();
    (void)hipFuncSetAttribute((const void*)gemm_kernel<BM, BN, WM, WN, BK, NS, EPI_RELU>, hipFuncAttributeMaxDynamicSharedMemorySize, SH);
    gemm_kernel<BM, BN, WM, WN, BK, NS, EPI_RELU><<<(S_ / BM) * (FF_ / BN), WM * WN * 64, SH, stream>>>(h2, w1, hid, M_, FF_, FF_ / 16, M_, FF_ / BN);
  }
  gemm_rs<14><<<256, 512, RS_SH, stream>>>(hid, w2t, out, x1, mod + 5 * 512);
}
```

```cpp
#include <hip/hip_runtime.h>
#include <hip/hip_bf16.h>
#include <stdint.h>

typedef unsigned short bf16_t;
typedef short s16x4 __attribute__((ext_vector_type(4)));
typedef short bf16x8 __attribute__((ext_vector_type(8)));
typedef float f32x16 __attribute__((ext_vector_type(16)));
typedef float f32x4 __attribute__((ext_vector_type(4)));
typedef float f32x2 __attribute__((ext_vector_type(2)));
typedef unsigned u32x4 __attribute__((ext_vector_type(4)));
typedef unsigned u32x2 __attribute__((ext_vector_type(2)));
typedef __bf16 bfx2 __attribute__((ext_vector_type(2)));
typedef __attribute__((address_space(3))) s16x4* lds_s16x4_ptr;
typedef __attribute__((address_space(3))) unsigned* lds_u32_ptr;

#if defined(__HIP_DEVICE_COMPILE__)
#define NOPK __attribute__((target("no-packed-fp32-ops")))
#else
#define NOPK
#endif
#define DI NOPK __device__ __forceinline__

constexpr int S_ = 2048, M_ = 512, H_ = 32, D_ = 128, HD_ = 4096, FF_ = 1792;

DI float u2f(unsigned u) { return __builtin_bit_cast(float, u); }
DI unsigned f2u(float f) { return __builtin_bit_cast(unsigned, f); }
#define SYNC() asm volatile("s_waitcnt vmcnt(0) lgkmcnt(0)\n\ts_barrier" ::: "memory")
DI float shx(float v, int o) {
  const int lane = __builtin_amdgcn_mbcnt_hi(~0u, __builtin_amdgcn_mbcnt_lo(~0u, 0u));
  return u2f((unsigned)__builtin_amdgcn_ds_bpermute((lane ^ o) << 2, (int)f2u(v)));
}
DI bool any_lane(bool p) { return __builtin_amdgcn_ballot_w64(p) != 0ull; }
DI unsigned pk_bf16(float lo, float hi) {
  f32x2 v = {lo, hi};
  bfx2 r = __builtin_convertvector(v, bfx2);
  return __builtin_bit_cast(unsigned, r);
}
DI float bf_lo(unsigned u) { return u2f(u << 16); }
DI float bf_hi(unsigned u) { return u2f(u & 0xffff0000u); }
DI float bf2f(bf16_t v) { return u2f(((unsigned)v) << 16); }
DI float rbf(float x) { return bf_lo(pk_bf16(x, 0.f)); }
DI f32x16 mfma32(bf16x8 a, bf16x8 b, f32x16 c) { return __builtin_amdgcn_mfma_f32_32x32x16_bf16(a, b, c, 0, 0, 0); }
#define PIN(x) asm volatile("" : "+v"(x))
DI bf16x8 tr2(const char* p0, const char* p1) {
  s16x4 a = __builtin_amdgcn_ds_read_tr16_b64_v4i16((lds_s16x4_ptr)(p0));
  s16x4 b = __builtin_amdgcn_ds_read_tr16_b64_v4i16((lds_s16x4_ptr)(p1));
  return __builtin_shufflevector(a, b, 0, 1, 2, 3, 4, 5, 6, 7);
}
DI void glds16(const void* g, char* l) {
  __builtin_amdgcn_global_load_lds((const unsigned*)g, (lds_u32_ptr)l, 16, 0, 0);
}

NOPK __global__ __launch_bounds__(256) void ln_kernel(const bf16_t* __restrict__ x, const float* __restrict__ shift, const float* __restrict__ scale, bf16_t* __restrict__ h) {
  const int lane = threadIdx.x & 63, row = 256 * (blockIdx.x & 7) + 4 * (blockIdx.x >> 3) + (threadIdx.x >> 6);
  u32x4 v = *(const u32x4*)(x + (size_t)row * M_ + lane * 8);
  float f[8];
#pragma unroll
  for (int j = 0; j < 4; ++j) { f[2 * j] = bf_lo(v[j]); f[2 * j + 1] = bf_hi(v[j]); }
  float s = 0;
#pragma unroll
  for (int j = 0; j < 8; ++j) s += f[j];
#pragma unroll
  for (int o = 32; o; o >>= 1) s += shx(s, o);
  const float mean = s * (1.f / M_);
  float q = 0;
#pragma unroll
  for (int j = 0; j < 8; ++j) { f[j] -= mean; q += f[j] * f[j]; }
#pragma unroll
  for (int o = 32; o; o >>= 1) q += shx(q, o);
  const float rstd = rsqrtf(q * (1.f / M_) + 1e-5f);
  u32x4 o;
#pragma unroll
  for (int j = 0; j < 4; ++j) {
    const int n = lane * 8 + 2 * j;
    float a = f[2 * j] * rstd * rbf(1.f + scale[n]) + shift[n];
    float b = f[2 * j + 1] * rstd * rbf(1.f + scale[n + 1]) + shift[n + 1];
    o[j] = pk_bf16(a, b);
  }
  *(u32x4*)(h + (size_t)row * M_ + lane * 8) = o;
}

enum { EPI_PLAIN = 0, EPI_RELU = 1, EPI_RES = 2 };
#define WAIT_VM(n) asm volatile("s_waitcnt vmcnt(%0)" ::"i"(n) : "memory")
#define WAIT_LGKM0() asm volatile("s_waitcnt lgkmcnt(0)" ::: "memory")

struct NoSide { DI void pre() {} DI void post() {} };
template <int BM, int BN, int WM, int WN, int BK, int NS, int EPI, class Side = NoSide>
DI void gemm_body(const bf16_t* __restrict__ A, int lda, const bf16_t* __restrict__ B, int ldb, bf16_t* __restrict__ C, int ldc, int K,
                  int m0, int n0, char* smem, Side side = Side()) {
  constexpr int NW = WM * WN;
  constexpr int WTM = BM / WM, WTN = BN / WN, TM = WTM / 32, TN = WTN / 32;
  constexpr int AROW = BK * 2, BROW = BN * 2;
  constexpr int A_BYTES = BM * AROW, B_BYTES = BK * BROW, BUF = A_BYTES + B_BYTES;
  constexpr int A_INSTR = A_BYTES / 1024, B_INSTR = B_BYTES / 1024;
  static_assert(A_INSTR % NW == 0 && B_INSTR % NW == 0, "staging split");
  constexpr int PER = (A_INSTR + B_INSTR) / NW;
  constexpr int ACH = AROW / 16;
  const int tid = threadIdx.x, lane = tid & 63, w = __builtin_amdgcn_readfirstlane(tid >> 6);
  const int wm = w / WN, wn = w % WN, r32 = lane & 31, hi = lane >> 5;

  const bf16_t* asrc[A_INSTR / NW];
  const bf16_t* bsrc[B_INSTR / NW];
#pragma unroll
  for (int i = 0; i < A_INSTR / NW; ++i) {
    const int piece = w + i * NW, u = piece * 64 + lane, r = u / ACH, cc = u % ACH;
    const int ch = cc ^ (BK == 64 ? ((r >> 1) & 7) : ((r >> 2) & 3));
    asrc[i] = A + (size_t)(m0 + r) * lda + ch * 8;
  }
  constexpr int UPR = BN / 8;
#pragma unroll
  for (int i = 0; i < B_INSTR / NW; ++i) {
    const int piece = w + i * NW, u = piece * 64 + lane, r = u / UPR, uu = u % UPR;
    const int c = (uu >> 2) ^ (BN >= 128 ? (r & 3) : ((r >> 1) & 1));
    bsrc[i] = B + (size_t)r * ldb + n0 + (c * 4 + (uu & 3)) * 8;
  }
  auto stage = [&](int t, int buf) NOPK {
    char* base = smem + buf * BUF;
#pragma unroll
    for (int i = 0; i < A_INSTR / NW; ++i) glds16(asrc[i] + t * BK, base + (w + i * NW) * 1024);
#pragma unroll
    for (int i = 0; i < B_INSTR / NW; ++i) glds16(bsrc[i] + (size_t)t * BK * ldb, base + A_BYTES + (w + i * NW) * 1024);
  };

  f32x16 acc[TM][TN];
#pragma unroll
  for (int i = 0; i < TM; ++i)
#pragma unroll
    for (int j = 0; j < TN; ++j)
#pragma unroll
      for (int r = 0; r < 16; ++r) acc[i][j][r] = 0.f;
#pragma unroll
  for (int i = 0; i < TM; ++i)
#pragma unroll
    for (int j = 0; j < TN; ++j) PIN(acc[i][j]);

  const int asw = BK == 64 ? ((lane >> 1) & 7) : ((lane >> 2) & 3);
  const int a_off = (wm * WTM + r32) * AROW;
  const int q = (lane & 15) >> 2, p = lane & 3, chalf = (lane >> 4) & 1;
  const int bsw = BN >= 128 ? q : (q >> 1);
  const int b_row = (8 * hi + q) * BROW + (chalf * 16 + 4 * p) * 2;
  const int nt = K / BK;
#pragma unroll
  for (int t = 0; t < NS - 1; ++t) stage(t, t);
  side.pre();
#define PP_BAR() asm volatile("s_barrier" ::: "memory")
  static_assert(WM == 2, "ping-pong needs two wave groups");
  constexpr int KSN = BK / 16;
  bf16x8 fa[KSN][TM], fb[KSN][TN];
  WAIT_VM(PER * (NS - 2));
  PP_BAR();
  if (wm == 1) PP_BAR();
  int buf = 0;
  for (int t = 0; t < nt; ++t) {
    {
      const char* As = smem + buf * BUF;
      const char* Bs = As + A_BYTES;
#pragma unroll
      for (int s = 0; s < KSN; ++s) {
#pragma unroll
        for (int i = 0; i < TM; ++i) fa[s][i] = *(const bf16x8*)(As + a_off + i * 32 * AROW + (((2 * s + hi) ^ asw) << 4));
#pragma unroll
        for (int j = 0; j < TN; ++j) {
          const int cj = (wn * WTN) / 32 + j;
          const char* pb = Bs + b_row + ((cj ^ bsw) << 6) + (16 * s) * BROW;
          const unsigned pa_ = (unsigned)(uintptr_t)pb;
          s16x4 lo_, hi_;
          asm volatile("ds_read_b64_tr_b16 %0, %1" : "=v"(lo_) : "v"(pa_));
          asm volatile("ds_read_b64_tr_b16 %0, %1 offset:%2" : "=v"(hi_) : "v"(pa_), "i"(4 * BROW));
          fb[s][j] = __builtin_shufflevector(lo_, hi_, 0, 1, 2, 3, 4, 5, 6, 7);
        }
      }
      if (t + NS - 1 < nt) { int nb = buf + NS - 1; if (nb >= NS) nb -= NS; stage(t + NS - 1, nb); }
    }
    if (wm == 1) { if (t + NS - 1 < nt) WAIT_VM(PER * (NS - 2)); else WAIT_VM(0); }
    asm volatile("s_waitcnt lgkmcnt(0)\n\ts_barrier" ::: "memory");
#pragma unroll
    for (int s = 0; s < KSN; ++s) {
#pragma unroll
      for (int i = 0; i < TM; ++i) asm volatile("" : "+v"(fa[s][i]));
#pragma unroll
      for (int j = 0; j < TN; ++j) asm volatile("" : "+v"(fb[s][j]));
    }
    __builtin_amdgcn_sched_barrier(0);
    __builtin_amdgcn_s_setprio(1);
#pragma unroll
    for (int s = 0; s < KSN; ++s)
#pragma unroll
      for (int i = 0; i < TM; ++i)
#pragma unroll
        for (int j = 0; j < TN; ++j) acc[i][j] = mfma32(fb[s][j], fa[s][i], acc[i][j]);
    __builtin_amdgcn_s_setprio(0);
    __builtin_amdgcn_sched_barrier(0);
    if (wm == 0) { if (t + NS - 1 < nt) WAIT_VM(PER * (NS - 2)); else WAIT_VM(0); }
    PP_BAR();
    ++buf; if (buf == NS) buf = 0;
  }
  if (wm == 0) PP_BAR();
  SYNC();
  constexpr int ERS = WTN * 2 + 16;
  char* ep = smem + w * (WTM * ERS);
#pragma unroll
  for (int i = 0; i < TM; ++i)
#pragma unroll
    for (int j = 0; j < TN; ++j)
#pragma unroll
      for (int gq = 0; gq < 4; ++gq) {
        float v0 = acc[i][j][4 * gq], v1 = acc[i][j][4 * gq + 1], v2 = acc[i][j][4 * gq + 2], v3 = acc[i][j][4 * gq + 3];
        if (EPI == EPI_RELU) { v0 = fmaxf(v0, 0.f); v1 = fmaxf(v1, 0.f); v2 = fmaxf(v2, 0.f); v3 = fmaxf(v3, 0.f); }
        u32x2 o = {pk_bf16(v0, v1), pk_bf16(v2, v3)};
        *(u32x2*)(ep + (i * 32 + r32) * ERS + (j * 32 + 8 * gq + 4 * hi) * 2) = o;
      }
  constexpr int LPR = WTN / 8, RPI = 64 / LPR;
  const int er = lane / LPR, ec = lane % LPR;
#pragma unroll
  for (int it = 0; it < WTM / RPI; ++it) {
    const int r = it * RPI + er;
    u32x4 v = *(const u32x4*)(ep + r * ERS + ec * 16);
    if (EPI == EPI_RELU) {
      const int row = m0 + wm * WTM + r, ch = (n0 + wn * WTN) / 8 + ec;
      *(u32x4*)(C + ((size_t)((row >> 5) * ldc + (ch >> 1)) * 64 + (row & 31) + 32 * (ch & 1)) * 8) = v;
    } else {
      *(u32x4*)(C + (size_t)(m0 + wm * WTM + r) * ldc + n0 + wn * WTN + ec * 8) = v;
    }
  }
  side.post();
}

template <int BM, int BN, int WM, int WN, int BK, int NS, int EPI>
constexpr int gemm_smem() {
  constexpr int a = NS * (BM * BK * 2 + BK * BN * 2), b = WM * WN * (BM / WM) * ((BN / WN) * 2 + 16);
  return a > b ? a : b;
}

template <int BM, int BN, int WM, int WN, int BK, int NS, int EPI>
NOPK __global__ __launch_bounds__(WM* WN * 64) void gemm_kernel(const bf16_t* A, const bf16_t* B, bf16_t* C, int lda, int ldb, int ldc, int K, int ntn) {
  extern __shared__ __attribute__((aligned(16))) char smem[];
  const int idx = blockIdx.x >> 3, bm = 2 * (blockIdx.x & 7) + idx / ntn, bn = idx % ntn;
  gemm_body<BM, BN, WM, WN, BK, NS, EPI>(A, lda, B, ldb, C, ldc, K, bm * BM, bn * BN, smem);
}

struct KvSide {
  const bf16_t* c; const bf16_t* wmod; float* mod; bf16_t* modb; const bf16_t* x; float* stats; bf16_t* xt; char* scratch;
  u32x4 xv, w0, w1; bf16_t cb;
  DI void pre() {
    const int tid = threadIdx.x, lane = tid & 63, w = tid >> 6, b = blockIdx.x;
    xv = *(const u32x4*)(x + (size_t)(b * 8 + w) * M_ + lane * 8);
    const int bb = b < 192 ? b : 0;
    cb = c[tid];
    w0 = *(const u32x4*)(wmod + (size_t)tid * 3072 + bb * 16); w1 = *(const u32x4*)(wmod + (size_t)tid * 3072 + bb * 16 + 8);
  }
  DI void post() {
    const int tid = threadIdx.x, lane = tid & 63, w = tid >> 6, b = blockIdx.x;
    {
      const int row = b * 8 + w;
      *(u32x4*)(xt + ((size_t)((row >> 5) * 32 + (lane >> 1)) * 64 + (row & 31) + 32 * (lane & 1)) * 8) = xv;
      float f[8];
#pragma unroll
      for (int j = 0; j < 4; ++j) { f[2 * j] = bf_lo(xv[j]); f[2 * j + 1] = bf_hi(xv[j]); }
      float sm = 0;
#pragma unroll
      for (int j = 0; j < 8; ++j) sm += f[j];
#pragma unroll
      for (int o = 32; o; o >>= 1) sm += shx(sm, o);
      const float mean = sm * (1.f / M_);
      float q = 0;
#pragma unroll
      for (int j = 0; j < 8; ++j) { const float d = f[j] - mean; q += d * d; }
#pragma unroll
      for (int o = 32; o; o >>= 1) q += shx(q, o);
      if (lane == 0) { f32x2 st = {mean, rsqrtf(q * (1.f / M_) + 1e-5f)}; *(f32x2*)(stats + 2 * row) = st; }
    }
    if (b < 192) {
      const float cv = bf2f(cb);
      const float sv = rbf(cv / (1.f + __expf(-cv)));
      float a[16];
#pragma unroll
      for (int j = 0; j < 4; ++j) { a[2 * j] = sv * bf_lo(w0[j]); a[2 * j + 1] = sv * bf_hi(w0[j]); a[8 + 2 * j] = sv * bf_lo(w1[j]); a[8 + 2 * j + 1] = sv * bf_hi(w1[j]); }
      float b8[8], b4[4], b2[2], b1;
      {
        const bool up = lane & 32;
#pragma unroll
        for (int j = 0; j < 8; ++j) { const float keep = up ? a[8 + j] : a[j], give = up ? a[j] : a[8 + j]; b8[j] = keep + shx(give, 32); }
      }
      {
        const bool up = lane & 16;
#pragma unroll
        for (int j = 0; j < 4; ++j) { const float keep = up ? b8[4 + j] : b8[j], give = up ? b8[j] : b8[4 + j]; b4[j] = keep + shx(give, 16); }
      }
      {
        const bool up = lane & 8;
#pragma unroll
        for (int j = 0; j < 2; ++j) { const float keep = up ? b4[2 + j] : b4[j], give = up ? b4[j] : b4[2 + j]; b2[j] = keep + shx(give, 8); }
      }
      {
        const bool up = lane & 4;
        const float keep = up ? b2[1] : b2[0], give = up ? b2[0] : b2[1];
        b1 = keep + shx(give, 4);
      }
      b1 += shx(b1, 2);
      b1 += shx(b1, 1);
      SYNC();
      float* red = (float*)scratch;
      if ((lane & 3) == 0) red[w * 16 + (lane >> 2)] = b1;
      SYNC();
      if (tid < 16) {
        float t = 0;
#pragma unroll
        for (int i = 0; i < 8; ++i) t += red[i * 16 + tid];
        const int n = b * 16 + tid;
        const float r = rbf(t);
        mod[n] = r;
        float* mf = (float*)modb;
        if (n < 512) mf[n] = r;
        else if (n < 1024) mf[n] = rbf(1.f + r);
        else if (n >= 1536 && n < 2048) mf[n - 512] = r;
        else if (n >= 2048 && n < 2560) mf[n - 512] = rbf(1.f + r);
      }
    }
  }
};
template <int BM, int BN, int WM, int WN, int BK, int NS>
NOPK __global__ __launch_bounds__(WM* WN * 64) void kv_kernel(const bf16_t* ckv, const bf16_t* wk, const bf16_t* wv, bf16_t* k, bf16_t* v,
                                                          const bf16_t* c, const bf16_t* wmod, float* mod, bf16_t* modb, const bf16_t* x, float* stats, bf16_t* xt) {
  extern __shared__ __attribute__((aligned(16))) char smem[];
  constexpr int NTN = HD_ / BN, NTM = S_ / BM, PER = NTN * NTM;
  static_assert(2 * PER == 256 && WM * WN == 8, "side jobs assume 256 blocks of 512 threads");
  const int xcd = blockIdx.x & 7, idx = blockIdx.x >> 3;
  const int which = idx >> 4, b = (2 * xcd + ((idx >> 3) & 1)) * NTM + (idx & 7);
  const bf16_t* B = which == 0 ? wk : wv;
  bf16_t* C = which == 0 ? k : v;
  KvSide side; side.c = c; side.wmod = wmod; side.mod = mod; side.modb = modb; side.x = x; side.stats = stats; side.xt = xt; side.scratch = smem;
  gemm_body<BM, BN, WM, WN, BK, NS, EPI_PLAIN, KvSide>(ckv, M_, B, HD_, C, HD_, M_, (b % NTM) * BM, (b / NTM) * BN, smem, side);
}

template <int NKS>
NOPK __global__ __launch_bounds__(512) void gemm_rs(const bf16_t* __restrict__ At, const bf16_t* __restrict__ Bt, bf16_t* __restrict__ C,
                                                    const bf16_t* __restrict__ res, const float* __restrict__ gate) {
  extern __shared__ __attribute__((aligned(16))) char smem[];
  constexpr int KS = NKS * 8;
  constexpr int D = NKS < 8 ? NKS : 8;
  const int tid = threadIdx.x, lane = tid & 63, w = __builtin_amdgcn_readfirstlane(tid >> 6);
  const int r32 = lane & 31, hi = lane >> 5;
  const int xcd = blockIdx.x & 7, idx = blockIdx.x >> 3;
  const int mb = xcd * 4 + (idx >> 3), nb = idx & 7;
  const auto ra = __builtin_amdgcn_make_buffer_rsrc((void*)At, 0, S_ * KS * 32, 0x00020000);
  const auto rb = __builtin_amdgcn_make_buffer_rsrc((void*)Bt, 0, KS * 16 * M_ * 2, 0x00020000);
  const int vo = lane * 16;
  const int sa0 = ((2 * mb) * KS + w * NKS) * 1024, sa1 = sa0 + KS * 1024;
  const int sb0 = ((w * NKS) * 16 + 2 * nb) * 1024;
  f32x16 acc[2][2];
#pragma unroll
  for (int i = 0; i < 2; ++i)
#pragma unroll
    for (int j = 0; j < 2; ++j)
#pragma unroll
      for (int r = 0; r < 16; ++r) acc[i][j][r] = 0.f;
  PIN(acc[0][0]); PIN(acc[0][1]); PIN(acc[1][0]); PIN(acc[1][1]);
  u32x4 fa[D][2], fb[D][2];
#define RS_LOAD(SL, T) do { \
    fa[SL][0] = __builtin_amdgcn_raw_buffer_load_b128(ra, vo, sa0 + (T) * 1024, 0); \
    fb[SL][0] = __builtin_amdgcn_raw_buffer_load_b128(rb, vo, sb0 + (T) * 16384, 0); \
    fa[SL][1] = __builtin_amdgcn_raw_buffer_load_b128(ra, vo, sa1 + (T) * 1024, 0); \
    fb[SL][1] = __builtin_amdgcn_raw_buffer_load_b128(rb, vo, sb0 + (T) * 16384 + 1024, 0); } while (0)
#pragma unroll
  for (int t = 0; t < D; ++t) RS_LOAD(t, t);
  __builtin_amdgcn_sched_barrier(0);
#pragma unroll
  for (int t = 0; t < NKS; ++t) {
    const int sl = t % D;
#pragma unroll
    for (int i = 0; i < 2; ++i)
#pragma unroll
      for (int j = 0; j < 2; ++j)
        acc[i][j] = mfma32(__builtin_bit_cast(bf16x8, fb[sl][j]), __builtin_bit_cast(bf16x8, fa[sl][i]), acc[i][j]);
    __builtin_amdgcn_sched_barrier(0);
    if (t + D < NKS) RS_LOAD(sl, t + D);
    __builtin_amdgcn_sched_barrier(0);
  }
  constexpr int RS = 272;
  {
    char* pw = smem + w * (64 * RS);
#pragma unroll
    for (int i = 0; i < 2; ++i)
#pragma unroll
      for (int j = 0; j < 2; ++j)
#pragma unroll
        for (int gq = 0; gq < 4; ++gq) {
          f32x4 v = {acc[i][j][4 * gq], acc[i][j][4 * gq + 1], acc[i][j][4 * gq + 2], acc[i][j][4 * gq + 3]};
          *(f32x4*)(pw + (i * 32 + r32) * RS + (j * 32 + 8 * gq + 4 * hi) * 4) = v;
        }
  }
  SYNC();
  {
    const int m0 = mb * 64, n0 = nb * 64;
    const int m = tid >> 3, nn = (tid & 7) * 8;
    f32x4 s0 = {0, 0, 0, 0}, s1 = {0, 0, 0, 0};
#pragma unroll
    for (int pp = 0; pp < 8; ++pp) {
      const char* src = smem + pp * (64 * RS) + m * RS + nn * 4;
      s0 += *(const f32x4*)src; s1 += *(const f32x4*)(src + 16);
    }
    const u32x4 rr = *(const u32x4*)(res + (size_t)(m0 + m) * M_ + n0 + nn);
    const f32x4 g0 = *(const f32x4*)(gate + n0 + nn), g1 = *(const f32x4*)(gate + n0 + nn + 4);
    u32x4 o;
    o[0] = pk_bf16(bf_lo(rr[0]) + rbf(g0[0] * rbf(s0[0])), bf_hi(rr[0]) + rbf(g0[1] * rbf(s0[1])));
    o[1] = pk_bf16(bf_lo(rr[1]) + rbf(g0[2] * rbf(s0[2])), bf_hi(rr[1]) + rbf(g0[3] * rbf(s0[3])));
    o[2] = pk_bf16(bf_lo(rr[2]) + rbf(g1[0] * rbf(s1[0])), bf_hi(rr[2]) + rbf(g1[1] * rbf(s1[1])));
    o[3] = pk_bf16(bf_lo(rr[3]) + rbf(g1[2] * rbf(s1[2])), bf_hi(rr[3]) + rbf(g1[3] * rbf(s1[3])));
    *(u32x4*)(C + (size_t)(m0 + m) * M_ + n0 + nn) = o;
  }
}

constexpr float ATT_C2 = 0.08838834764831845f * 1.4426950408889634f;
#define DSR128(dst, addr, off) asm volatile("ds_read_b128 %0, %1 offset:%2" : "=v"(dst) : "v"(addr), "i"(off))
#define DSRTR(dst, addr, off) asm volatile("ds_read_b64_tr_b16 %0, %1 offset:%2" : "=v"(dst) : "v"(addr), "i"(off))
#define SB0() __builtin_amdgcn_sched_barrier(0)
DI void glds_s(const char* sbase, unsigned voff, unsigned lds_addr) {
  asm volatile("s_mov_b32 m0, %2\n\ts_nop 0\n\tglobal_load_lds_dwordx4 %0, %1" ::"v"(voff), "s"(sbase), "s"(lds_addr) : "memory");
}
#define K_READ2(F, S0) do { const unsigned a0_ = kaddr ^ ((S0) << 5), a1_ = kaddr ^ (((S0) + 1) << 5); \
    DSR128(F[0], a0_, 0); DSR128(F[1], a0_, 8192); DSR128(F[2], a1_, 0); DSR128(F[3], a1_, 8192); } while (0)
#define WAIT4(N, F) asm volatile("s_waitcnt lgkmcnt(" #N ")" : "+v"(F[0]), "+v"(F[1]), "+v"(F[2]), "+v"(F[3]))
#define K_MMA2(F, S0) do { \
    _Pragma("unroll") for (int s_ = 0; s_ < 2; ++s_) { s0 = mfma32(F[2 * s_], qr[S0 + s_], s0); s1 = mfma32(F[2 * s_ + 1], qr[S0 + s_], s1); } } while (0)
#define ATT_QK(HOOK) do { \
  bf16x8 fa[4], fb[4]; \
  K_READ2(fa, 0); \
  K_READ2(fb, 2); \
  if (last) { int dq_ = dq_last; asm volatile("" : "+v"(dq_)); \
    _Pragma("unroll") for (int r = 0; r < 16; ++r) { const int c_ = (r & 3) + 8 * (r >> 2); \
      s0[r] = c_ > dq_ ? -__builtin_inff() : -m; s1[r] = c_ + 32 > dq_ ? -__builtin_inff() : -m; } \
  } else { _Pragma("unroll") for (int r = 0; r < 16; ++r) { s0[r] = -m; s1[r] = -m; } } \
  WAIT4(4, fa); SB0(); \
  K_MMA2(fa, 0); SB0(); \
  K_READ2(fa, 4); HOOK(0); \
  WAIT4(4, fb); SB0(); \
  K_MMA2(fb, 2); SB0(); \
  K_READ2(fb, 6); HOOK(1); \
  WAIT4(4, fa); SB0(); \
  K_MMA2(fa, 4); SB0(); HOOK(2); \
  WAIT4(0, fb); SB0(); \
  K_MMA2(fb, 6); SB0(); HOOK(3); } while (0)
struct VFrag { s16x4 lo[4], hi[4]; };
#define V_READ1(F, S) do { \
      DSRTR(F.lo[0], va0, (S) * 4096); DSRTR(F.hi[0], va0, (S) * 4096 + 2048); \
      DSRTR(F.lo[1], va1, (S) * 4096); DSRTR(F.hi[1], va1, (S) * 4096 + 2048); \
      DSRTR(F.lo[2], va2, (S) * 4096); DSRTR(F.hi[2], va2, (S) * 4096 + 2048); \
      DSRTR(F.lo[3], va3, (S) * 4096); DSRTR(F.hi[3], va3, (S) * 4096 + 2048); } while (0)
#define VWAIT(N, F) asm volatile("s_waitcnt lgkmcnt(" #N ")" : "+v"(F.lo[0]), "+v"(F.lo[1]), "+v"(F.lo[2]), "+v"(F.lo[3]), "+v"(F.hi[0]), "+v"(F.hi[1]), "+v"(F.hi[2]), "+v"(F.hi[3]))
#define V_MMA1(F, S) do { const bf16x8 pb_ = __builtin_bit_cast(bf16x8, pw[S]); \
      _Pragma("unroll") for (int d_ = 0; d_ < 4; ++d_) \
        o[d_] = mfma32(__builtin_shufflevector(F.lo[d_], F.hi[d_], 0, 1, 2, 3, 4, 5, 6, 7), pb_, o[d_]); } while (0)
#define V_ADDR() const unsigned va0 = vaddr, va1 = vaddr ^ 64, va2 = vaddr ^ 128, va3 = vaddr ^ 192
#define PV_TAIL(HOOK) do { \
      VWAIT(8, f0); SB0(); V_MMA1(f0, 0); SB0(); V_READ1(f0, 2); HOOK(0); \
      VWAIT(8, f1); SB0(); V_MMA1(f1, 1); SB0(); V_READ1(f1, 3); HOOK(1); \
      VWAIT(8, f0); SB0(); V_MMA1(f0, 2); SB0(); HOOK(2); \
      VWAIT(0, f1); SB0(); V_MMA1(f1, 3); SB0(); HOOK(3); } while (0)
#define ATT_SM(HOOK) do { \
  HOOK(0); \
  float mx = s0[0]; \
  _Pragma("unroll") for (int r = 1; r < 16; ++r) mx = fmaxf(mx, s0[r]); \
  _Pragma("unroll") for (int r = 0; r < 16; ++r) mx = fmaxf(mx, s1[r]); \
  { auto rr = __builtin_amdgcn_permlane32_swap(f2u(mx), f2u(mx), false, false); \
    mx = fmaxf(u2f(rr[0]), u2f(rr[1])); } \
    \
    \
  if (it == 0 || any_lane(mx > 8.f)) { \
    const float delta = it == 0 ? mx : fmaxf(mx, 0.f); \
    m += delta; \
    if (it != 0) { const float alpha = __builtin_amdgcn_exp2f(-delta); l *= alpha; \
      _Pragma("unroll") for (int d = 0; d < 4; ++d) _Pragma("unroll") for (int r = 0; r < 16; ++r) o[d][r] *= alpha; } \
    _Pragma("unroll") for (int r = 0; r < 16; ++r) { s0[r] -= delta; s1[r] -= delta; } } \
  float ps = 0.f; \
  SB0(); HOOK(1); SB0(); \
  _Pragma("unroll") for (int r = 0; r < 8; ++r) { s0[r] = __builtin_amdgcn_exp2f(s0[r]); ps += s0[r]; } \
  SB0(); HOOK(2); SB0(); \
  _Pragma("unroll") for (int r = 8; r < 16; ++r) { s0[r] = __builtin_amdgcn_exp2f(s0[r]); ps += s0[r]; } \
  SB0(); HOOK(3); SB0(); \
  _Pragma("unroll") for (int r = 0; r < 8; ++r) { s1[r] = __builtin_amdgcn_exp2f(s1[r]); ps += s1[r]; } \
  SB0(); HOOK(4); SB0(); \
  _Pragma("unroll") for (int r = 8; r < 16; ++r) { s1[r] = __builtin_amdgcn_exp2f(s1[r]); ps += s1[r]; } \
  SB0(); HOOK(5); SB0(); \
  l += ps; \
  _Pragma("unroll") for (int s = 0; s < 4; ++s) _Pragma("unroll") for (int j = 0; j < 4; ++j) { \
      const int r = 8 * (s & 1) + 2 * j; \
      pw[s][j] = s < 2 ? pk_bf16(s0[r], s0[r + 1]) : pk_bf16(s1[r], s1[r + 1]); } } while (0)
#define ATT_BAR() asm volatile("s_waitcnt vmcnt(0) lgkmcnt(0)\n\ts_barrier" ::: "memory")
#define NOHOOK(i) do {} while (0)

NOPK __global__ __launch_bounds__(512) void attn_kernel(const bf16_t* __restrict__ X, const float* __restrict__ stats, const bf16_t* __restrict__ modb,
                                                   const bf16_t* __restrict__ Wq, const bf16_t* __restrict__ Kg, const bf16_t* __restrict__ Vg, bf16_t* __restrict__ ctx,
                                                   const bf16_t* __restrict__ Wo, const bf16_t* __restrict__ W2, bf16_t* __restrict__ Wot, bf16_t* __restrict__ W2t) {
  extern __shared__ __attribute__((aligned(16))) char smem[];
  const int tid = threadIdx.x, lane = tid & 63, w = __builtin_amdgcn_readfirstlane(tid >> 6), g = w >> 2, wq = w & 3;
  const int r32 = lane & 31, hi = lane >> 5;
  const int b = blockIdx.x, head = 4 * (b & 7) + (b >> 6), pair = (b >> 3) & 7;
  const unsigned lds0 = (unsigned)(uintptr_t)smem;
  const unsigned Kl = lds0 + g * 65536, Vl = Kl + 32768;
  const int srow = 4 * wq + (lane >> 4), su = lane & 15;
  const unsigned ksrc = (unsigned)(srow * HD_ + head * D_ + ((su ^ srow) << 3)) * 2u;
  const unsigned vsrc = (unsigned)(srow * HD_ + head * D_ + (((((su >> 2) ^ (srow & 3)) << 2) | (su & 3)) << 3)) * 2u;
#define STAGE_K1(tile, buf, i) glds_s((const char*)Kg + (size_t)((tile) * 64 + 16 * (i)) * (HD_ * 2), ksrc, Kl + (buf) * 16384 + wq * 1024 + (i) * 4096)
#define STAGE_V1(tile, buf, i) glds_s((const char*)Vg + (size_t)((tile) * 64 + 16 * (i)) * (HD_ * 2), vsrc, Vl + (buf) * 16384 + wq * 1024 + (i) * 4096)
  const int q4 = (lane & 15) >> 2, p4 = lane & 3, chalf = (lane >> 4) & 1;
  const unsigned kaddr0 = Kl + r32 * 256 + ((hi ^ (r32 & 15)) << 4);
  const unsigned vaddr0 = Vl + (4 * hi + q4) * 256 + (chalf * 16 + 4 * p4) * 2 + (q4 << 6);

  bf16x8 qr[8];
  char* Qs = smem + 131072;
  {
    {
      const int kr = lane >> 4;
      const unsigned wsrc = (unsigned)(kr * HD_ + head * D_ + (((((su >> 2) ^ kr) << 2) | (su & 3)) << 3)) * 2u;
#pragma unroll
      for (int i = 0; i < 16; ++i) glds_s((const char*)Wq + (size_t)((w * 16 + i) * 4) * (HD_ * 2), wsrc, lds0 + (w * 16 + i) * 1024);
    }
    const int boff = (8 * hi + q4) * 256 + (chalf * 16 + 4 * p4) * 2;
    const int xq = g == 0 ? 15 - pair : pair;
    const f32x2 stq = *(const f32x2*)(stats + 2 * (128 * xq + 32 * wq + r32));
    const float rs = stq[1], nm = -stq[0] * stq[1];
    const auto rx = __builtin_amdgcn_make_buffer_rsrc((void*)X, 0, S_ * M_ * 2, 0x00020000);
    const int voX = lane * 16;
    const int soX = (4 * xq + wq) * 32768;
    if (w < 4) glds_s((const char*)modb + w * 1024, (unsigned)voX, lds0 + 131072 + w * 1024);
    f32x16 qa[4];
#pragma unroll
    for (int r = 0; r < 16; ++r) { qa[0][r] = 0.f; qa[1][r] = 0.f; qa[2][r] = 0.f; qa[3][r] = 0.f; }
    u32x4 xa[8];
#pragma unroll
    for (int t = 0; t < 8; ++t) xa[t] = __builtin_amdgcn_raw_buffer_load_b128(rx, voX, soX + t * 1024, 0);
    ATT_BAR();
    unsigned short re[3][8];
#pragma unroll
    for (int i = 0; i < 3; ++i) {
      const int f0 = (blockIdx.x * 8 + w) + i * 2048, f = f0 < 4096 + 1792 ? f0 : 0;
      const bool first = f < 4096;
      const int ff = first ? f : f - 4096;
      const bf16_t* src = (first ? Wo : W2) + (size_t)((ff >> 4) * 16 + 8 * hi) * M_ + (ff & 15) * 32 + r32;
#pragma unroll
      for (int j = 0; j < 8; ++j) re[i][j] = src[(size_t)j * M_];
    }
    const unsigned wb_lo = lds0 + boff + (q4 << 6), wb_hi = wb_lo + 65536;
    s16x4 wl[2][4], wh[2][4];
#define W_READ(B_, KS_) do { const unsigned a_ = (KS_) < 16 ? wb_lo : wb_hi; \
      { DSRTR(wl[B_][0], a_, ((KS_) & 15) * 4096); DSRTR(wh[B_][0], a_, ((KS_) & 15) * 4096 + 1024); } \
      { const unsigned aj_ = a_ ^ 64; DSRTR(wl[B_][1], aj_, ((KS_) & 15) * 4096); DSRTR(wh[B_][1], aj_, ((KS_) & 15) * 4096 + 1024); } \
      { const unsigned aj_ = a_ ^ 128; DSRTR(wl[B_][2], aj_, ((KS_) & 15) * 4096); DSRTR(wh[B_][2], aj_, ((KS_) & 15) * 4096 + 1024); } \
      { const unsigned aj_ = a_ ^ 192; DSRTR(wl[B_][3], aj_, ((KS_) & 15) * 4096); DSRTR(wh[B_][3], aj_, ((KS_) & 15) * 4096 + 1024); } } while (0)
    const unsigned cfa = lds0 + 131072 + hi * 32;
    u32x4 cf[2][4];
#define C_READ(B_, KS_) do { DSR128(cf[B_][0], cfa, (KS_) * 64); DSR128(cf[B_][1], cfa, (KS_) * 64 + 16); \
      DSR128(cf[B_][2], cfa, 2048 + (KS_) * 64); DSR128(cf[B_][3], cfa, 2048 + (KS_) * 64 + 16); } while (0)
#define W_WAIT(N, B_) asm volatile("s_waitcnt lgkmcnt(" #N ")" : "+v"(wl[B_][0]), "+v"(wl[B_][1]), "+v"(wl[B_][2]), "+v"(wl[B_][3]), "+v"(wh[B_][0]), "+v"(wh[B_][1]), "+v"(wh[B_][2]), "+v"(wh[B_][3]), \
      "+v"(cf[B_][0]), "+v"(cf[B_][1]), "+v"(cf[B_][2]), "+v"(cf[B_][3]))
#define QP_STEP(KS_) do { \
        if ((KS_) + 1 < 32) { C_READ(((KS_) & 1) ^ 1, ((KS_) + 1) & 31); W_READ(((KS_) & 1) ^ 1, ((KS_) + 1) & 31); } \
        if ((KS_) + 1 < 32) W_WAIT(12, (KS_) & 1); else W_WAIT(0, (KS_) & 1); \
        u32x4 ha; \
        _Pragma("unroll") for (int j = 0; j < 4; ++j) \
          ha[j] = pk_bf16(fmaf(fmaf(bf_lo(xa[(KS_) & 7][j]), rs, nm), u2f(cf[(KS_) & 1][2 + (j >> 1)][(2 * j) & 3]), u2f(cf[(KS_) & 1][j >> 1][(2 * j) & 3])), \
                          fmaf(fmaf(bf_hi(xa[(KS_) & 7][j]), rs, nm), u2f(cf[(KS_) & 1][2 + (j >> 1)][(2 * j + 1) & 3]), u2f(cf[(KS_) & 1][j >> 1][(2 * j + 1) & 3]))); \
        const bf16x8 fa_ = __builtin_bit_cast(bf16x8, ha); \
        SB0(); \
        if ((KS_) + 8 < 32) xa[(KS_) & 7] = __builtin_amdgcn_raw_buffer_load_b128(rx, voX, soX + ((KS_) + 8) * 1024, 0); \
        _Pragma("unroll") for (int j = 0; j < 4; ++j) qa[j] = mfma32(__builtin_shufflevector(wl[(KS_) & 1][j], wh[(KS_) & 1][j], 0, 1, 2, 3, 4, 5, 6, 7), fa_, qa[j]); \
        SB0(); } while (0)
#define QP_4(K0) do { QP_STEP(K0); QP_STEP(K0 + 1); QP_STEP(K0 + 2); QP_STEP(K0 + 3); } while (0)
    C_READ(0, 0); W_READ(0, 0);
    QP_4(0); QP_4(4); QP_4(8); QP_4(12); QP_4(16); QP_4(20); QP_4(24); QP_4(28);
#pragma unroll
    for (int i = 0; i < 3; ++i) {
      const int f = (blockIdx.x * 8 + w) + i * 2048;
      if (f < 4096 + 1792) {
        const bool first = f < 4096;
        const int ff = first ? f : f - 4096;
        u32x4 pk;
#pragma unroll
        for (int j = 0; j < 4; ++j) pk[j] = (unsigned)re[i][2 * j] | ((unsigned)re[i][2 * j + 1] << 16);
        *(u32x4*)((first ? Wot : W2t) + ((size_t)ff * 64 + lane) * 8) = pk;
      }
    }
    asm volatile("s_waitcnt lgkmcnt(0)\n\ts_barrier" ::: "memory");
#pragma unroll
    for (int i = 0; i < 4; ++i) STAGE_K1(g, 0, i);
    if (g == 0) {
#pragma unroll
      for (int i = 0; i < 4; ++i) STAGE_V1(0, 0, i);
    }
    const int lr = 32 * wq + r32;
    {
      char* dst = g == 0 ? smem + 81920 : Qs;
#pragma unroll
      for (int j = 0; j < 4; ++j)
#pragma unroll
        for (int gq = 0; gq < 4; ++gq) {
          const int d = 32 * j + 8 * gq + 4 * hi;
          u32x2 ov = {pk_bf16(qa[j][4 * gq] * ATT_C2, qa[j][4 * gq + 1] * ATT_C2), pk_bf16(qa[j][4 * gq + 2] * ATT_C2, qa[j][4 * gq + 3] * ATT_C2)};
          *(u32x2*)(dst + lr * 256 + ((((d >> 3) ^ (lr & 15))) << 4) + (d & 7) * 2) = ov;
        }
    }
    asm volatile("s_waitcnt lgkmcnt(0)\n\ts_barrier" ::: "memory");
#pragma unroll
    for (int s = 0; s < 8; ++s) qr[s] = *(const bf16x8*)(smem + 81920 + lr * 256 + (((2 * s + hi) ^ (lr & 15)) << 4));
  }

  unsigned kaddr = kaddr0, vaddr = vaddr0;
  int p0 = 0;
#pragma nounroll
  for (int pass = 0; pass < 2; ++pass) {
    const int x = pass == 0 ? 15 - pair : pair;
    const int nt = x + 1;
    const int qrow = 128 * x + 32 * wq + r32;
    f32x16 o[4], s0, s1;
    u32x4 pw[4];
    float m = 0.f, l = 0.f;
    if (pass == 1) {
      const int lr = 32 * wq + r32;
#pragma unroll
      for (int s = 0; s < 8; ++s) qr[s] = *(const bf16x8*)(Qs + lr * 256 + (((2 * s + hi) ^ (lr & 15)) << 4));
    }
#pragma unroll
    for (int d = 0; d < 4; ++d)
#pragma unroll
      for (int r = 0; r < 16; ++r) o[d][r] = 0.f;
    if (pass == 0) ATT_BAR();
    const int dq_last = qrow - 64 * (2 * (nt - 1) + g) - 4 * hi;
    if (g == 0) {
#pragma nounroll
      for (int it = 0; it < nt; ++it) {
        const int buf = (it + p0) & 1;
        const bool last = it + 1 == nt;
        const int nx = last ? 0 : 2 * (it + 1);
#define HOOK_A(i) do { STAGE_K1(nx, buf ^ 1, i); STAGE_V1(nx, buf ^ 1, i); } while (0)
        ATT_QK(HOOK_A);
        VFrag f0, f1;
        V_ADDR();
        V_READ1(f0, 0); V_READ1(f1, 1);
#define BK1(i) glds_s((const char*)Kg + (size_t)((nx + 1) * 64 + 16 * (i)) * (HD_ * 2), ksrc, Kl + 65536 + (buf ^ 1) * 16384 + wq * 1024 + (i) * 4096)
#define BV1(i) glds_s((const char*)Vg + (size_t)((2 * it + 1) * 64 + 16 * (i)) * (HD_ * 2), vsrc, Vl + 65536 + buf * 16384 + wq * 1024 + (i) * 4096)
#define HOOK_SM(i) do { if ((i) == 0) { BV1(0); BV1(1); } else if ((i) == 1) { BV1(2); BV1(3); } else if ((i) == 2) BK1(0); else if ((i) == 3) BK1(1); else if ((i) == 4) BK1(2); else BK1(3); } while (0)
        SB0();
        ATT_SM(HOOK_SM);
        SB0();
        PV_TAIL(NOHOOK);
        kaddr ^= 16384; vaddr ^= 16384;
        ATT_BAR();
      }
    } else {
      const bool skip_last = wq < 2;
#pragma nounroll
      for (int it = 0; it < nt; ++it) {
        const int buf = (it + p0) & 1;
        const bool last = it + 1 == nt;
        if (it > 0) {
          VFrag f0, f1;
          V_ADDR();
          V_READ1(f0, 0); V_READ1(f1, 1);
          PV_TAIL(NOHOOK);
          vaddr ^= 16384;
        }
        if (!(last && skip_last)) {
          ATT_QK(NOHOOK);
          ATT_SM(NOHOOK);
        }
        kaddr ^= 16384;
        ATT_BAR();
      }
      if (!skip_last) {
        VFrag f0, f1;
        V_ADDR();
        V_READ1(f0, 0); V_READ1(f1, 1);
        PV_TAIL(NOHOOK);
      }
      vaddr ^= 16384;
    }
    ATT_BAR();
    const int pl = (nt - 1 + p0) & 1;
    l += shx(l, 32);
    if (l == 0.f) m = -1e30f;
    float* xo = (float*)(smem + g * 65536 + (wq >> 1) * 32768 + pl * 16384 + (wq & 1) * 8192);
    float* sp = (float*)(smem + 65536 + 32768 + (pl ^ 1) * 16384 + w * 512);
#pragma unroll
    for (int dd = 0; dd < 2; ++dd)
#pragma unroll
      for (int r = 0; r < 16; ++r) xo[(dd * 16 + r) * 64 + lane] = g == 0 ? o[2 + dd][r] : o[dd][r];
    sp[lane] = m; sp[64 + lane] = l;
    SYNC();
    {
      const float* po = (const float*)(smem + (g ^ 1) * 65536 + (wq >> 1) * 32768 + pl * 16384 + (wq & 1) * 8192);
      const float* pst = (const float*)(smem + 65536 + 32768 + (pl ^ 1) * 16384 + (w ^ 4) * 512);
      const float mo = pst[lane], lo = pst[64 + lane];
      const float mt = fmaxf(m, mo);
      const float a1 = __builtin_amdgcn_exp2f(m - mt), a2 = __builtin_amdgcn_exp2f(mo - mt);
      const float inv = 1.f / (l * a1 + lo * a2);
      const float f1 = a1 * inv, f2 = a2 * inv;
      bf16_t* obase = ctx + ((size_t)((qrow >> 5) * 256 + head * 8 + g * 4) * 64 + r32) * 8 + 4 * hi;
#pragma unroll
      for (int dd = 0; dd < 2; ++dd)
#pragma unroll
        for (int gq = 0; gq < 4; ++gq) {
          float v[4];
#pragma unroll
          for (int e = 0; e < 4; ++e) {
            const int r = 4 * gq + e;
            const float own = g == 0 ? o[dd][r] : o[2 + dd][r];
            v[e] = own * f1 + po[(dd * 16 + r) * 64 + lane] * f2;
          }
          u32x2 ov = {pk_bf16(v[0], v[1]), pk_bf16(v[2], v[3])};
          *(u32x2*)(obase + ((dd * 2 + (gq >> 1)) * 64 + 32 * (gq & 1)) * 8) = ov;
        }
    }
    asm volatile("s_waitcnt lgkmcnt(0)\n\ts_barrier" ::: "memory");
    p0 = (p0 + nt) & 1;
  }
}

extern "C" void kernel_launch(void* const* d_in, const int* in_sizes, int n_in,
                              void* d_out, int out_size, void* d_ws, size_t ws_size,
                              hipStream_t stream) {
  const bf16_t* x = (const bf16_t*)d_in[0];
  const bf16_t* ckv = (const bf16_t*)d_in[1];
  const bf16_t* wq = (const bf16_t*)d_in[2];
  const bf16_t* wuk = (const bf16_t*)d_in[3];
  const bf16_t* wuv = (const bf16_t*)d_in[4];
  const bf16_t* wo = (const bf16_t*)d_in[5];
  const bf16_t* w1 = (const bf16_t*)d_in[7];
  const bf16_t* w2 = (const bf16_t*)d_in[8];
  const bf16_t* c = (const bf16_t*)d_in[9];
  const bf16_t* wmod = (const bf16_t*)d_in[10];
  bf16_t* out = (bf16_t*)d_out;

  char* ws = (char*)d_ws;
  float* mod = (float*)ws;
  bf16_t* h1 = (bf16_t*)(ws + (1 << 20));
  bf16_t* x1 = (bf16_t*)(ws + (4 << 20));
  bf16_t* h2 = (bf16_t*)(ws + (7 << 20));
  bf16_t* hid = (bf16_t*)(ws + (10 << 20));
  bf16_t* q = (bf16_t*)(ws + (32 << 20));
  bf16_t* k = (bf16_t*)(ws + (48 << 20));
  bf16_t* v = (bf16_t*)(ws + (64 << 20));
  bf16_t* ctx = (bf16_t*)(ws + (80 << 20));
  bf16_t* wot = (bf16_t*)(ws + (96 << 20));
  bf16_t* w2t = (bf16_t*)(ws + (100 << 20));

  bf16_t* modb = (bf16_t*)(ws + (32 << 10));
  float* stats = (float*)(ws + (64 << 10));
  {
        constexpr int BM = 256, BN = 256, WM = 2, WN = 4, BK = 32, NS = 4, SH = gemm_smem<BM, BN, WM, WN, BK, NS, 0>();
    (void)hipFuncSetAttribute((const void*)kv_kernel<BM, BN, WM, WN, BK, NS>, hipFuncAttributeMaxDynamicSharedMemorySize, SH);
    kv_kernel<BM, BN, WM, WN, BK, NS><<<256, WM * WN * 64, SH, stream>>>(ckv, wuk, wuv, k, v, c, wmod, mod, modb, x, stats, h1);
  }
  (void)hipFuncSetAttribute((const void*)attn_kernel, hipFuncAttributeMaxDynamicSharedMemorySize, 163840);
  attn_kernel<<<256, 512, 163840, stream>>>(h1, stats, modb, wq, k, v, ctx, wo, w2, wot, w2t);
  constexpr int RS_SH = 8 * 64 * 272;
  (void)hipFuncSetAttribute((const void*)gemm_rs<32>, hipFuncAttributeMaxDynamicSharedMemorySize, RS_SH);
  (void)hipFuncSetAttribute((const void*)gemm_rs<14>, hipFuncAttributeMaxDynamicSharedMemorySize, RS_SH);
  gemm_rs<32><<<256, 512, RS_SH, stream>>>(ctx, wot, x1, x, mod + 2 * 512);
  ln_kernel<<<S_ / 4, 256, 0, stream>>>(x1, mod + 3 * 512, mod + 4 * 512, h2);
  {
        constexpr int BM = 128, BN = 128, WM = 2, WN = 4, BK = 64, NS = 4, SH = gemm_smem<BM, BN, WM, WN, BK, NS, 0>();
    (void)hipFuncSetAttribute((const void*)gemm_kernel<BM, BN, WM, WN, BK, NS, EPI_RELU>, hipFuncAttributeMaxDynamicSharedMemorySize, SH);
    gemm_kernel<BM, BN, WM, WN, BK, NS, EPI_RELU><<<(S_ / BM) * (FF_ / BN), WM * WN * 64, SH, stream>>>(h2, w1, hid, M_, FF_, FF_ / 16, M_, FF_ / BN);
  }
  gemm_rs<14><<<256, 512, RS_SH, stream>>>(hid, w2t, out, x1, mod + 5 * 512);
}
```
